```python
import torch

def _softmax(x, axis=-1):
    return torch.softmax(x, dim=axis)
def _gelu(x):
    return torch.nn.functional.gelu(x, approximate="tanh")
def _silu(x):
    return torch.nn.functional.silu(x)
def _sigmoid(x):
    return torch.sigmoid(x)
def _layernorm(x, axis=-1, eps=1e-5):
    m = x.mean(dim=axis, keepdim=True)
    v = x.var(dim=axis, keepdim=True, unbiased=False)
    return (x - m) / torch.sqrt(v + eps)
def _scatter_add(tgt, idx, src, axis=0):
    return tgt.clone().index_add_(axis, idx, src)
def _segment_sum(src, seg, n):
    out = torch.zeros((n,) + src.shape[1:], dtype=src.dtype)
    return out.index_add_(0, seg, src)
def _pad(x, pw):
    flat = tuple(v for pair in reversed(pw) for v in pair)
    return torch.nn.functional.pad(x, flat)

kernel_name = 'interleave_layers__grouped_kv_mqa__moe_arch_mod__prompt'

def setup_inputs():

    _w = (lambda a: (lambda t: t.to(torch.bfloat16) if t.is_floating_point() else t)(torch.as_tensor(a)))
    def _A_setup():
        B, S, D, H = 1, 2048, 576, 32
        Dh = D // H
        return {
            "x": _w((lambda *s: torch.randn(*s, dtype=torch.bfloat16) * 0.05)(B, S, D)),
            "wq": _w((lambda *s: torch.randn(*s, dtype=torch.bfloat16) * 0.05)(D, H * Dh)),
            "wk": _w((lambda *s: torch.randn(*s, dtype=torch.bfloat16) * 0.05)(D, Dh)),
            "wv": _w((lambda *s: torch.randn(*s, dtype=torch.bfloat16) * 0.05)(D, Dh)),
            "wo": _w((lambda *s: torch.randn(*s, dtype=torch.bfloat16) * 0.05)(H * Dh, D)),
        }
    _A_in = _A_setup()

    def _B_setup():
        B, S, D, F, C = 1, 2048, 576, 4608, 1
        return {
            "x": _w((lambda *s: torch.randn(*s, dtype=torch.bfloat16) * 0.05)(B, S, D)),
            "w_r": _w((lambda *s: torch.randn(*s, dtype=torch.bfloat16) * 0.05)(D)),
            "w_gate": _w((lambda *s: torch.randn(*s, dtype=torch.bfloat16) * 0.05)(D, F)),
            "w_up": _w((lambda *s: torch.randn(*s, dtype=torch.bfloat16) * 0.05)(D, F)),
            "w_down": _w((lambda *s: torch.randn(*s, dtype=torch.bfloat16) * 0.05)(F, D)),
        }
    _B_in = _B_setup()

    return {
        "A_x": _A_in["x"],
        "A_wq": _A_in["wq"],
        "A_wk": _A_in["wk"],
        "A_wv": _A_in["wv"],
        "A_wo": _A_in["wo"],
        "B_x": _B_in["x"],
        "B_w_r": _B_in["w_r"],
        "B_w_gate": _B_in["w_gate"],
        "B_w_up": _B_in["w_up"],
        "B_w_down": _B_in["w_down"],
    }

def reference(A_x, A_wq, A_wk, A_wv, A_wo, B_x, B_w_r, B_w_gate, B_w_up, B_w_down):
    def _A_ref(x, wq, wk, wv, wo):
        B, S, D = x.shape
        H = 32
        Dh = D // H
        q = (lambda x, *d: x.permute(*d))((x @ wq).reshape(B, S, H, Dh), 0, 2, 1, 3)
        k = x @ wk
        v = x @ wv
        scores = torch.einsum("bhqd,bkd->bhqk", q, k) * (Dh ** -0.5)
        p = _softmax(scores, axis=-1)
        o = torch.einsum("bhqk,bkd->bhqd", p, v)
        return (lambda x, *d: x.permute(*d))(o, 0, 2, 1, 3).reshape(B, S, H * Dh) @ wo

    def _B_ref(x, w_r, w_gate, w_up, w_down):
        B, S, D = x.shape
        C = 1
        r = x @ w_r
        idx = torch.argsort(-r, axis=-1)[:, :C]
        idx_e = (lambda x, n, axis: torch.repeat_interleave(x, n, dim=axis))(idx.reshape(B, C, 1), D, axis=2)
        x_sel = (lambda x, i, axis: torch.take_along_dim(x, i, dim=axis))(x, idx_e, axis=1)
        g = _silu(x_sel @ w_gate)
        u = x_sel @ w_up
        y = (g * u) @ w_down
        r_sel = _sigmoid((lambda x, i, axis: torch.take_along_dim(x, i, dim=axis))(r, idx, axis=1))
        y = y * r_sel.reshape(B, C, 1)
        b_off = (torch.arange(B) * S).reshape(B, 1)
        flat_idx = (idx + b_off).reshape(B * C)
        zero = (x * 0.0).reshape(B * S, D)
        upd = _scatter_add(zero, flat_idx, y.reshape(B * C, D), axis=0)
        return x + upd.reshape(B, S, D)

    x = A_x
    x = x + _A_ref(_layernorm(x, axis=-1), A_wq, A_wk, A_wv, A_wo)
    x = x + _B_ref(_layernorm(x, axis=-1), B_w_r, B_w_gate, B_w_up, B_w_down)
    x = x + _A_ref(_layernorm(x, axis=-1), A_wq, A_wk, A_wv, A_wo)
    x = x + _B_ref(_layernorm(x, axis=-1), B_w_r, B_w_gate, B_w_up, B_w_down)
    return x
```

```cpp
#include <hip/hip_runtime.h>
#include <hip/hip_bf16.h>
#include <stdint.h>

typedef __bf16 bf16x8 __attribute__((ext_vector_type(8)));
typedef __bf16 bf16x16 __attribute__((ext_vector_type(16)));
typedef float f32x16 __attribute__((ext_vector_type(16)));
typedef float f32x8 __attribute__((ext_vector_type(8)));
typedef float f32x4 __attribute__((ext_vector_type(4)));
typedef unsigned u32x4 __attribute__((ext_vector_type(4)));
typedef unsigned u32x2 __attribute__((ext_vector_type(2)));
typedef unsigned short u16;
#define LAS __attribute__((address_space(3)))
#define GAS __attribute__((address_space(1)))
#define DI __device__ __forceinline__

constexpr int S = 2048, D = 576, F = 4608;
constexpr int VSUB = 320;
constexpr int TILE_B = 1024 + 128 + 4 * VSUB;
constexpr int KV_B = 64 * TILE_B;
constexpr int K2_OFF = 1024, V_OFF = 1152;
constexpr float QSCALE = 0.2357022603955158f * 1.4426950408889634f;

constexpr size_t WS_WTQKV = 0;
constexpr size_t WS_WTO = WS_WTQKV + 640 * 576 * 2;
constexpr size_t WS_QF = WS_WTO + 576 * 576 * 2;
constexpr size_t WS_KV = WS_QF + (size_t)2048 * 1152;
constexpr size_t WS_O = WS_KV + KV_B + 8192;
constexpr size_t WS_X2 = WS_O + (size_t)S * D * 2;
constexpr size_t WS_XN = WS_X2 + (size_t)S * D * 4;
constexpr size_t WS_R = WS_XN + (size_t)S * D * 2;
constexpr size_t WS_SLAB = WS_R + S * 4;
constexpr size_t WS_END = WS_SLAB + 144 * 576 * 4;

#define SBAR() __builtin_amdgcn_sched_barrier(0)
#define PIN(x) asm volatile("" : "+v"(x))
#define MFMA32(a, b, c) __builtin_amdgcn_mfma_f32_32x32x16_bf16((a), (b), (c), 0, 0, 0)
#define MFMA16(a, b, c) __builtin_amdgcn_mfma_f32_16x16x32_bf16((a), (b), (c), 0, 0, 0)
DI f32x16 pv(bf16x8 v, f32x8 p, f32x16 o) { return __builtin_amdgcn_smfmac_f32_32x32x32_bf16(v, __builtin_bit_cast(bf16x16, p), o, (int)0xDDDDDDDDu, 0, 0); }
DI f32x8 lo8(const f32x16& x) { return __builtin_shufflevector(x, x, 0, 1, 2, 3, 4, 5, 6, 7); }
DI f32x8 hi8(const f32x16& x) { return __builtin_shufflevector(x, x, 8, 9, 10, 11, 12, 13, 14, 15); }
DI float bf2f(u16 b) { return __uint_as_float((unsigned)b << 16); }
DI u16 f2bf(float f) { return __builtin_bit_cast(u16, (__bf16)f); }
DI void glds16(const void* g, unsigned lds_base) {
    unsigned sv; asm volatile("s_mov_b32 %0, m0\n\ts_mov_b32 m0, %2\n\ts_nop 0\n\tglobal_load_lds_dwordx4 %1, off\n\ts_mov_b32 m0, %0" : "=&s"(sv) : "v"(g), "s"(lds_base) : "memory"); }

__global__ void kprep(const u16* wq, const u16* wk, const u16* wv, const u16* wo, u16* wtqkv, u16* wto) {
    const int i = blockIdx.x * blockDim.x + threadIdx.x;
    if (i < 640 * 576) { const int n = i / 576, k = i % 576; u16 v = 0; if (n < 576) v = wq[k * 576 + n]; else if (n < 594) v = wk[k * 18 + n - 576]; else if (n < 612) v = wv[k * 18 + n - 594]; wtqkv[i] = v; }
    if (i < 576 * 576) { const int n = i / 576, k = i % 576; wto[i] = wo[k * 576 + n]; }
}

template <bool F32IN>
__global__ __launch_bounds__(256) void kqkv(const void* xin, const u16* wt, char* qf, char* kv) {
    __shared__ __attribute__((aligned(16))) u16 xs[16 * 584];
    const int tid = threadIdx.x, lane = tid & 63, wave = tid >> 6, T0 = blockIdx.x * 16;
    { const int row = tid >> 4, j = tid & 15; float v[36]; float s = 0.f;
      for (int i = 0; i < 36; ++i) { const int c = i * 16 + j; v[i] = F32IN ? ((const float*)xin)[(size_t)(T0 + row) * D + c] : bf2f(((const u16*)xin)[(size_t)(T0 + row) * D + c]); s += v[i]; }
      for (int o = 1; o < 16; o <<= 1) s += __shfl_xor(s, o);
      const float mu = s * (1.f / D); float q = 0.f;
      for (int i = 0; i < 36; ++i) { v[i] -= mu; q += v[i] * v[i]; }
      for (int o = 1; o < 16; o <<= 1) q += __shfl_xor(q, o);
      const float rs = rsqrtf(q * (1.f / D) + 1e-5f);
      for (int i = 0; i < 36; ++i) xs[row * 584 + i * 16 + j] = f2bf(v[i] * rs); }
    __syncthreads();
    const int n16 = lane & 15, g = lane >> 4;
    for (int ct = wave; ct < 39; ct += 4) {
        f32x4 acc = {0, 0, 0, 0};
        for (int s = 0; s < 18; ++s) {
            const bf16x8 a = *(const bf16x8*)(xs + n16 * 584 + s * 32 + g * 8);
            const bf16x8 b = *(const bf16x8*)(wt + (size_t)(ct * 16 + n16) * 576 + s * 32 + g * 8);
            acc = MFMA16(a, b, acc);
        }
        const int n = ct * 16 + n16;
        for (int j = 0; j < 4; ++j) { const int T = T0 + 4 * g + j; const float val = acc[j];
            if (n < 576) { const int H = n / 18, d = n % 18, gg = T >> 6, m = H >> 2, p = H & 3, tl = (T & 63) >> 5, r = T & 31;
                const int b = (gg & 7) + 8 * (m + 8 * (gg >> 3)); char* t = qf + ((size_t)b * 8 + p * 2 + tl) * 1152;
                const int off = d < 16 ? ((d >> 3) * 32 + r) * 16 + (d & 7) * 2 : 1024 + r * 4 + (d - 16) * 2;
                *(u16*)(t + off) = f2bf(val * QSCALE); }
            else if (n < 594) { const int d = n - 576, r = T & 31; char* t = kv + (size_t)(T >> 5) * TILE_B;
                const int off = d < 16 ? ((d >> 3) * 32 + r) * 16 + (d & 7) * 2 : K2_OFF + r * 4 + (d - 16) * 2; *(u16*)(t + off) = f2bf(val); }
            else if (n < 614) { const int d = n - 594, r = T & 31; char* t = kv + (size_t)(T >> 5) * TILE_B;
                *(u16*)(t + V_OFF + (r >> 3) * VSUB + d * 16 + (r & 7) * 2) = d < 18 ? f2bf(val) : (d == 18 ? (u16)0x3F80 : (u16)0); }
        }
    }
}

struct FragQ { bf16x8 a1, a2, b1, b2; };
template <bool WITHMAX>
DI void attn_pass(LAS const char* lds, int kh, int lane, const FragQ& q, const f32x16& cia, const f32x16& cib, f32x16& oa, f32x16& ob) {
    const int r = lane & 31, h = lane >> 5;
    const int dsel = r < 19 ? r : 19;
    LAS const char* kp = lds + kh * TILE_B + lane * 16;
    LAS const char* k2p = lds + kh * TILE_B + K2_OFF + r * 4;
    LAS const char* vp = lds + kh * TILE_B + V_OFF + h * VSUB + dsel * 16;
#define LDK(i) (*(LAS const bf16x8*)(kp + (i) * 2 * TILE_B))
#define LDK2(i) (*(LAS const unsigned*)(k2p + (i) * 2 * TILE_B))
#define LDV(i, s) (*(LAS const bf16x8*)(vp + (i) * 2 * TILE_B + (s) * 2 * VSUB))
    bf16x8 k1 = LDK(0); u32x4 k2u = {LDK2(0), 0, 0, 0};
    f32x16 sa0, sb0, sa1, sb1;
    sa0 = MFMA32(k1, q.a1, cia); sa0 = MFMA32(__builtin_bit_cast(bf16x8, k2u), q.a2, sa0);
    sb0 = MFMA32(k1, q.b1, cib); sb0 = MFMA32(__builtin_bit_cast(bf16x8, k2u), q.b2, sb0);
    k1 = LDK(1); k2u[0] = LDK2(1);
    bf16x8 v0 = LDV(0, 0), v1 = LDV(0, 1), v1p;
    { u32x4 z = {0, 0, 0, 0}; v1p = __builtin_bit_cast(bf16x8, z); }
    sb1 = f32x16{};
    if (!WITHMAX && kh == 1) __builtin_amdgcn_s_setprio(1);
#define EX4(S_, j) S_[j] = __builtin_amdgcn_exp2f(S_[j]); S_[j + 1] = __builtin_amdgcn_exp2f(S_[j + 1]); S_[j + 2] = __builtin_amdgcn_exp2f(S_[j + 2]); S_[j + 3] = __builtin_amdgcn_exp2f(S_[j + 3]);
#define STEP(CA, CB, NA, NB, i) do { \
        ob = pv(v1p, hi8(NB), ob); SBAR(); EX4(CA, 0) PIN(CA); SBAR(); \
        NA = MFMA32(k1, q.a1, cia); SBAR(); EX4(CA, 4) PIN(CA); SBAR(); \
        NA = MFMA32(__builtin_bit_cast(bf16x8, k2u), q.a2, NA); SBAR(); EX4(CA, 8) PIN(CA); SBAR(); \
        oa = pv(v0, lo8(CA), oa); SBAR(); EX4(CA, 12) PIN(CA); SBAR(); \
        NB = MFMA32(k1, q.b1, cib); SBAR(); k1 = LDK((i) + 2); SBAR(); EX4(CB, 0) PIN(CB); SBAR(); \
        oa = pv(v1, hi8(CA), oa); SBAR(); EX4(CB, 4) PIN(CB); SBAR(); \
        NB = MFMA32(__builtin_bit_cast(bf16x8, k2u), q.b2, NB); SBAR(); k2u[0] = LDK2((i) + 2); SBAR(); EX4(CB, 8) PIN(CB); SBAR(); \
        ob = pv(v0, lo8(CB), ob); SBAR(); EX4(CB, 12) PIN(CB); SBAR(); \
        v1p = v1; v0 = LDV((i) + 1, 0); v1 = LDV((i) + 1, 1); SBAR(); \
    } while (0)
    for (int i = 0; i < 32; i += 2) {
        if (!WITHMAX && i == 22) __builtin_amdgcn_s_setprio(0);
        STEP(sa0, sb0, sa1, sb1, 0);
        STEP(sa1, sb1, sa0, sb0, 1);
        kp += 4 * TILE_B; k2p += 4 * TILE_B; vp += 4 * TILE_B;
    }
    ob = pv(v1p, hi8(sb1), ob);
#undef STEP
#undef LDK
#undef LDK2
#undef LDV
}
DI void attn_max(LAS const char* lds, int kh, int lane, const FragQ& q, float& ma, float& mb) {
    const int r = lane & 31; ma = -INFINITY; mb = -INFINITY; const f32x16 zero = {};
    for (int i = 0; i < 32; ++i) { LAS const char* t = lds + (2 * i + kh) * TILE_B;
        const bf16x8 k1 = *(LAS const bf16x8*)(t + lane * 16); u32x4 k2u = {*(LAS const unsigned*)(t + K2_OFF + r * 4), 0, 0, 0};
        f32x16 sa = MFMA32(k1, q.a1, zero); sa = MFMA32(__builtin_bit_cast(bf16x8, k2u), q.a2, sa);
        f32x16 sb = MFMA32(k1, q.b1, zero); sb = MFMA32(__builtin_bit_cast(bf16x8, k2u), q.b2, sb);
        for (int j = 0; j < 16; ++j) { ma = fmaxf(ma, sa[j]); mb = fmaxf(mb, sb[j]); } }
    ma = fmaxf(ma, __shfl_xor(ma, 32)); mb = fmaxf(mb, __shfl_xor(mb, 32));
}
DI bool attn_bad(const f32x16& o) {
    bool bad = false; const int h = (threadIdx.x >> 5) & 1;
    if (h == 0) { const float l = o[10]; bad = !(l >= 0x1p-60f && l <= 0x1p59f); }
    for (int j = 0; j < 10; ++j) bad |= !(fabsf(o[j]) < INFINITY);
    return bad;
}

constexpr int ATT_LDS = 160 * 1024;
__global__ __launch_bounds__(512, 2) void kattn(const char* qf, const char* kv, u16* o) {
    extern __shared__ __attribute__((aligned(16))) char lds[];
    const int tid = threadIdx.x, lane = tid & 63, r = lane & 31, h = lane >> 5;
    const int wave = __builtin_amdgcn_readfirstlane(tid >> 6), p = wave & 3, kh = wave >> 2;
    const int b = blockIdx.x, gg = (b & 7) + 8 * (b >> 6), m = (b >> 3) & 7;
    const char* qb = qf + ((size_t)b * 8 + p * 2) * 1152;
    FragQ q; q.a1 = *(const bf16x8*)(qb + lane * 16); q.b1 = *(const bf16x8*)(qb + 1152 + lane * 16);
    const unsigned qa2r = *(const unsigned*)(qb + 1024 + r * 4), qb2r = *(const unsigned*)(qb + 1152 + 1024 + r * 4);
    const unsigned lds0 = (unsigned)(uintptr_t)lds;
#pragma unroll
    for (int i = 0; i < 19; ++i) { const int c = i * 8 + wave; glds16(kv + (size_t)c * 1024 + lane * 16, __builtin_amdgcn_readfirstlane(lds0 + c * 1024)); }
    { u32x4 t; t[0] = h ? 0u : qa2r; t[1] = 0; t[2] = 0; t[3] = 0; q.a2 = __builtin_bit_cast(bf16x8, t); t[0] = h ? 0u : qb2r; q.b2 = __builtin_bit_cast(bf16x8, t); }
    asm volatile("s_waitcnt vmcnt(0)\n\ts_barrier" ::: "memory");
    f32x16 oa = {}, ob = {}; const f32x16 zero = {};
    attn_pass<false>((LAS const char*)lds, kh, lane, q, zero, zero, oa, ob);
    LAS float* scr = (LAS float*)(lds + KV_B);
    LAS unsigned* flg = (LAS unsigned*)(lds + KV_B + 4096);
    { const bool bad = __any(attn_bad(oa) || attn_bad(ob)); if (lane == 0) flg[wave] = bad; }
    __syncthreads();
    const u32x4 f0 = *(LAS const u32x4*)flg, f1 = *(LAS const u32x4*)(flg + 4);
    if (__builtin_amdgcn_readfirstlane(f0[0] | f0[1] | f0[2] | f0[3] | f1[0] | f1[1] | f1[2] | f1[3])) {
        float ma, mb; attn_max((LAS const char*)lds, kh, lane, q, ma, mb);
        if (h == 0) { scr[(wave * 2) * 32 + r] = ma; scr[(wave * 2 + 1) * 32 + r] = mb; }
        __syncthreads();
        ma = fmaxf(ma, scr[((wave ^ 4) * 2) * 32 + r]); mb = fmaxf(mb, scr[((wave ^ 4) * 2 + 1) * 32 + r]);
        f32x16 ca, cb; for (int j = 0; j < 16; ++j) { ca[j] = -ma; cb[j] = -mb; }
        oa = f32x16{}; ob = f32x16{};
        attn_pass<true>((LAS const char*)lds, kh, lane, q, ca, cb, oa, ob);
        __syncthreads();
    }
    LAS float* ex = (LAS float*)lds + wave * 12 * 64;
    { const f32x16& snd = kh ? oa : ob;
#pragma unroll
      for (int j = 0; j < 11; ++j) ex[j * 64 + lane] = snd[j]; }
    __syncthreads();
    LAS const float* rx = (LAS const float*)lds + (wave ^ 4) * 12 * 64;
    f32x16 of = kh ? ob : oa;
#pragma unroll
    for (int j = 0; j < 11; ++j) of[j] += rx[j * 64 + lane];
    const float l = __shfl(of[10], r); const float il = 1.f / l;
    const int T = gg * 64 + kh * 32 + r, H = m * 4 + p;
    u16* op = o + (size_t)T * D + H * 18;
#pragma unroll
    for (int j = 0; j < 8; ++j) { const int d = (j & 3) + 8 * (j >> 2) + 4 * h; op[d] = f2bf(of[j] * il); }
    if (h == 0) { op[16] = f2bf(of[8] * il); op[17] = f2bf(of[9] * il); }
}

template <bool LAST, bool F32IN>
__global__ __launch_bounds__(256) void kout(const u16* o, const u16* wto, const void* xin, const u16* wr, float* x2, u16* xn, float* rr, u16* out) {
    __shared__ __attribute__((aligned(16))) u16 os[16 * 584];
    __shared__ float x1s[16 * 580];
    const int tid = threadIdx.x, lane = tid & 63, wave = tid >> 6, T0 = blockIdx.x * 16;
    for (int i = tid; i < 16 * 72; i += 256) { const int row = i / 72, c = i % 72; *(u32x4*)(os + row * 584 + c * 8) = *(const u32x4*)(o + (size_t)(T0 + row) * D + c * 8); }
    __syncthreads();
    const int n16 = lane & 15, g = lane >> 4;
    for (int ct = wave; ct < 36; ct += 4) {
        f32x4 acc = {0, 0, 0, 0};
        for (int s = 0; s < 18; ++s) {
            const bf16x8 a = *(const bf16x8*)(os + n16 * 584 + s * 32 + g * 8);
            const bf16x8 b = *(const bf16x8*)(wto + (size_t)(ct * 16 + n16) * 576 + s * 32 + g * 8);
            acc = MFMA16(a, b, acc);
        }
        for (int j = 0; j < 4; ++j) { const int row = 4 * g + j, n = ct * 16 + n16; const size_t gi = (size_t)(T0 + row) * D + n;
            x1s[row * 580 + n] = acc[j] + (F32IN ? ((const float*)xin)[gi] : bf2f(((const u16*)xin)[gi])); }
    }
    __syncthreads();
    { const int row = tid >> 4, j = tid & 15; float v[36]; float s = 0.f;
      for (int i = 0; i < 36; ++i) { v[i] = x1s[row * 580 + i * 16 + j]; s += v[i]; }
      for (int o_ = 1; o_ < 16; o_ <<= 1) s += __shfl_xor(s, o_);
      const float mu = s * (1.f / D); float q = 0.f;
      for (int i = 0; i < 36; ++i) { const float c = v[i] - mu; q += c * c; }
      for (int o_ = 1; o_ < 16; o_ <<= 1) q += __shfl_xor(q, o_);
      const float rs = rsqrtf(q * (1.f / D) + 1e-5f); float dot = 0.f;
      for (int i = 0; i < 36; ++i) { const int c = i * 16 + j; const float n_ = (v[i] - mu) * rs; dot += n_ * bf2f(wr[c]); const size_t gi = (size_t)(T0 + row) * D + c;
          xn[gi] = f2bf(n_); if (LAST) out[gi] = f2bf(v[i] + n_); x2[gi] = v[i] + n_; }
      for (int o_ = 1; o_ < 16; o_ <<= 1) dot += __shfl_xor(dot, o_);
      if (j == 0) rr[T0 + row] = dot; }
}

DI int block_argmax(const float* rr, float& best, float* sv, int* si) {
    const int tid = threadIdx.x; float bv = -INFINITY; int bi = 0x7fffffff;
    for (int i = tid; i < S; i += blockDim.x) { const float v = rr[i]; if (v > bv || (v == bv && i < bi) || (bi == 0x7fffffff)) { bv = v; bi = i; } }
    for (int o = 1; o < 64; o <<= 1) { const float ov = __shfl_xor(bv, o); const int oi = __shfl_xor(bi, o); if (ov > bv || (ov == bv && oi < bi)) { bv = ov; bi = oi; } }
    if ((tid & 63) == 0) { sv[tid >> 6] = bv; si[tid >> 6] = bi; }
    __syncthreads();
    bv = sv[0]; bi = si[0]; for (int w = 1; w < (int)(blockDim.x >> 6); ++w) if (sv[w] > bv || (sv[w] == bv && si[w] < bi)) { bv = sv[w]; bi = si[w]; }
    best = bv; return bi;
}
__global__ __launch_bounds__(512) void kffn1(const float* rr, const u16* xn, const u16* wg, const u16* wu, const u16* wd, float* slab) {
    __shared__ float sv[8]; __shared__ int si[8]; __shared__ float xs[576]; __shared__ float red[2][8][32]; __shared__ float hs[32];
    const int tid = threadIdx.x, lane = tid & 63, wave = tid >> 6, c = blockIdx.x; float best;
    const int sel = block_argmax(rr, best, sv, si);
    for (int i = tid; i < 576; i += 512) xs[i] = bf2f(xn[(size_t)sel * D + i]);
    __syncthreads();
    float ag[8] = {}, au[8] = {}; const int cq = tid & 3;
    for (int k = tid >> 2; k < 576; k += 128) { const u32x4 a = *(const u32x4*)(wg + (size_t)k * F + c * 32 + cq * 8), b = *(const u32x4*)(wu + (size_t)k * F + c * 32 + cq * 8); const float x = xs[k];
        for (int j = 0; j < 4; ++j) { ag[2 * j] += x * __uint_as_float(a[j] << 16); ag[2 * j + 1] += x * __uint_as_float(a[j] & 0xFFFF0000u); au[2 * j] += x * __uint_as_float(b[j] << 16); au[2 * j + 1] += x * __uint_as_float(b[j] & 0xFFFF0000u); } }
    for (int j = 0; j < 8; ++j) for (int o = 4; o < 64; o <<= 1) { ag[j] += __shfl_xor(ag[j], o); au[j] += __shfl_xor(au[j], o); }
    if (lane < 4) for (int j = 0; j < 8; ++j) { red[0][wave][lane * 8 + j] = ag[j]; red[1][wave][lane * 8 + j] = au[j]; }
    __syncthreads();
    if (tid < 32) { float g = 0, u = 0; for (int w = 0; w < 8; ++w) { g += red[0][w][tid]; u += red[1][w][tid]; } hs[tid] = g / (1.f + __expf(-g)) * u; }
    __syncthreads();
    for (int n = tid; n < 576; n += 512) { float y = 0; for (int j = 0; j < 32; ++j) y += hs[j] * bf2f(wd[(size_t)(c * 32 + j) * D + n]); slab[c * 576 + n] = y; }
}
template <bool LAST>
__global__ __launch_bounds__(512) void kffn2(const float* rr, const float* slab, float* x2, u16* out) {
    __shared__ float sv[8]; __shared__ int si[8]; float best;
    const int sel = block_argmax(rr, best, sv, si); const float sg = 1.f / (1.f + __expf(-best));
    for (int n = threadIdx.x; n < 576; n += 512) { float y = 0; for (int c = 0; c < 144; ++c) y += slab[c * 576 + n]; const float v = x2[(size_t)sel * D + n] + y * sg; x2[(size_t)sel * D + n] = v; if (LAST) out[(size_t)sel * D + n] = f2bf(v); }
}

extern "C" void kernel_launch(void* const* d_in, const int* in_sizes, int n_in, void* d_out, int out_size, void* d_ws, size_t ws_size, hipStream_t stream) {
    const u16* x = (const u16*)d_in[0]; const u16* wq = (const u16*)d_in[1]; const u16* wk = (const u16*)d_in[2]; const u16* wv = (const u16*)d_in[3]; const u16* wo = (const u16*)d_in[4];
    const u16* wr = (const u16*)d_in[6]; const u16* wg = (const u16*)d_in[7]; const u16* wu = (const u16*)d_in[8]; const u16* wd = (const u16*)d_in[9];
    char* ws = (char*)d_ws; u16* out = (u16*)d_out;
    u16* wtqkv = (u16*)(ws + WS_WTQKV); u16* wto = (u16*)(ws + WS_WTO); char* qf = ws + WS_QF; char* kv = ws + WS_KV; u16* o = (u16*)(ws + WS_O);
    float* x2 = (float*)(ws + WS_X2); u16* xn = (u16*)(ws + WS_XN); float* rr = (float*)(ws + WS_R); float* slab = (float*)(ws + WS_SLAB);
    (void)hipFuncSetAttribute((const void*)kattn, hipFuncAttributeMaxDynamicSharedMemorySize, ATT_LDS);
    kprep<<<(640 * 576 + 255) / 256, 256, 0, stream>>>(wq, wk, wv, wo, wtqkv, wto);
    kqkv<false><<<128, 256, 0, stream>>>(x, wtqkv, qf, kv);
    kattn<<<256, 512, ATT_LDS, stream>>>(qf, kv, o);
    kout<false, false><<<128, 256, 0, stream>>>(o, wto, x, wr, x2, xn, rr, out);
    kffn1<<<144, 512, 0, stream>>>(rr, xn, wg, wu, wd, slab);
    kffn2<false><<<1, 512, 0, stream>>>(rr, slab, x2, out);
    kqkv<true><<<128, 256, 0, stream>>>(x2, wtqkv, qf, kv);
    kattn<<<256, 512, ATT_LDS, stream>>>(qf, kv, o);
    kout<true, true><<<128, 256, 0, stream>>>(o, wto, x2, wr, x2, xn, rr, out);
    kffn1<<<144, 512, 0, stream>>>(rr, xn, wg, wu, wd, slab);
    kffn2<true><<<1, 512, 0, stream>>>(rr, slab, x2, out);
}
```

```cpp
#include <hip/hip_runtime.h>
#include <hip/hip_bf16.h>
#include <stdint.h>

typedef __bf16 bf16x8 __attribute__((ext_vector_type(8)));
typedef __bf16 bf16x16 __attribute__((ext_vector_type(16)));
typedef short s16x4 __attribute__((ext_vector_type(4)));
typedef float f32x16 __attribute__((ext_vector_type(16)));
typedef float f32x8 __attribute__((ext_vector_type(8)));
typedef float f32x4 __attribute__((ext_vector_type(4)));
typedef unsigned u32x4 __attribute__((ext_vector_type(4)));
typedef unsigned u32x2 __attribute__((ext_vector_type(2)));
typedef unsigned short u16;
#define LAS __attribute__((address_space(3)))
#define GAS __attribute__((address_space(1)))
#define DI __device__ __forceinline__
#define RLX_AGENT __ATOMIC_RELAXED, __HIP_MEMORY_SCOPE_AGENT

constexpr int S = 2048, D = 576, F = 4608;
constexpr int VSUB = 320;
constexpr int TILE_B = 1024 + 128 + 4 * VSUB;
constexpr int KV_B = 64 * TILE_B;
constexpr int K2_OFF = 1024, V_OFF = 1152;
constexpr float QSCALE = 0.2357022603955158f * 1.4426950408889634f;
constexpr int FRAGS_B = 90 * 1024;
constexpr int XO_G = 72 * 64 * 16;

constexpr size_t QF_B = (size_t)2048 * 1152, KVP_B = KV_B + 8192;
constexpr size_t WS_FLAG = 0;
constexpr size_t WS_FF = 65536;
constexpr size_t WS_GROW = WS_FF + 2 * 144 * 128;
constexpr size_t WS_GKV = WS_GROW + 576 * 8;
constexpr size_t WS_GCAND = WS_GKV + 16 * 64 * 8;
constexpr size_t WS_GST = WS_GCAND + 8 * 32 * 8;
constexpr size_t WS_GCXN = WS_GST + 32 * 8 * 192 * 8;
constexpr size_t WS_GCX4 = WS_GCXN + 32 * 8 * 36 * 8;
constexpr size_t WS_ZEND = WS_GCX4 + 32 * 8 * 72 * 8;
constexpr size_t ZERO_B = 1u << 20;
static_assert(WS_ZEND <= ZERO_B, "zeroed region");
constexpr size_t WS_QF = ZERO_B;
constexpr size_t WS_KV = WS_QF + 2 * QF_B;
constexpr size_t WS_WOF = WS_KV + 2 * KVP_B;
constexpr size_t WS_WQF = WS_WOF + 8 * FRAGS_B;
constexpr size_t WS_XO = WS_WQF + 8 * FRAGS_B;
constexpr size_t WS_XX = WS_XO + 32 * XO_G;
constexpr size_t WS_X1 = WS_XX + 32 * XO_G;
constexpr size_t WS_X2 = WS_X1 + (size_t)S * D * 4;
constexpr size_t WS_XN = WS_X2 + (size_t)S * D * 4;
constexpr size_t WS_R = WS_XN + (size_t)S * D * 2;
constexpr size_t WS_CAND0 = WS_R;
constexpr size_t WS_SLAB = WS_R + S * 4;
constexpr size_t WS_END = WS_SLAB + 2 * 144 * 576 * 4;

struct Params {
    const u16 *x, *wq, *wk, *wv, *wo, *wr, *wg, *wu, *wd; u16* out; char* ws;
};

#define SBAR() __builtin_amdgcn_sched_barrier(0)
#define PIN(x) asm volatile("" : "+v"(x))
#define MFMA32(a, b, c) __builtin_amdgcn_mfma_f32_32x32x16_bf16((a), (b), (c), 0, 0, 0)
#define MFMA16(a, b, c) __builtin_amdgcn_mfma_f32_16x16x32_bf16((a), (b), (c), 0, 0, 0)
DI f32x16 pv(bf16x8 v, f32x8 p, f32x16 o) { return __builtin_amdgcn_smfmac_f32_32x32x32_bf16(v, __builtin_bit_cast(bf16x16, p), o, (int)0xDDDDDDDDu, 0, 0); }
DI f32x8 lo8(const f32x16& x) { return __builtin_shufflevector(x, x, 0, 1, 2, 3, 4, 5, 6, 7); }
DI f32x8 hi8(const f32x16& x) { return __builtin_shufflevector(x, x, 8, 9, 10, 11, 12, 13, 14, 15); }
DI float bf2f(u16 b) { return __uint_as_float((unsigned)b << 16); }
DI float bflo(unsigned u) { return __uint_as_float(u << 16); }
DI float bfhi(unsigned u) { return __uint_as_float(u & 0xFFFF0000u); }
DI u16 f2bf(float f) { return __builtin_bit_cast(u16, (__bf16)f); }
typedef __bf16 bf16x2 __attribute__((ext_vector_type(2)));
typedef float f32x2 __attribute__((ext_vector_type(2)));
DI unsigned pk2(float lo, float hi) { return __builtin_bit_cast(unsigned, __builtin_convertvector(f32x2{lo, hi}, bf16x2)); }
DI void glds16(const void* g, unsigned lds_base) {
    unsigned sv; asm volatile("s_mov_b32 %0, m0\n\ts_mov_b32 m0, %2\n\ts_nop 0\n\tglobal_load_lds_dwordx4 %1, off\n\ts_mov_b32 m0, %0" : "=&s"(sv) : "v"(g), "s"(lds_base) : "memory"); }
DI void glds16s(const char* sbase, unsigned voff, unsigned lds_base) {
    unsigned sv; asm volatile("s_mov_b32 %0, m0\n\ts_mov_b32 m0, %3\n\ts_nop 0\n\tglobal_load_lds_dwordx4 %1, %2\n\ts_mov_b32 m0, %0" : "=&s"(sv) : "v"(voff), "s"(sbase), "s"(lds_base) : "memory"); }
DI void glds4s(const char* sbase, unsigned voff, unsigned lds_base) {
    unsigned sv; asm volatile("s_mov_b32 %0, m0\n\ts_mov_b32 m0, %3\n\ts_nop 0\n\tglobal_load_lds_dword %1, %2\n\ts_mov_b32 m0, %0" : "=&s"(sv) : "v"(voff), "s"(sbase), "s"(lds_base) : "memory"); }
typedef short v4i16_t __attribute__((ext_vector_type(4)));
DI s16x4 trread(LAS const char* p) { return __builtin_bit_cast(s16x4, __builtin_amdgcn_ds_read_tr16_b64_v4i16((LAS v4i16_t*)p)); }
DI float xg_sum(float x) {
    auto s = __builtin_amdgcn_permlane16_swap(__float_as_uint(x), __float_as_uint(x), false, false);
    x = __uint_as_float(s[0]) + __uint_as_float(s[1]);
    auto t = __builtin_amdgcn_permlane32_swap(__float_as_uint(x), __float_as_uint(x), false, false);
    return __uint_as_float(t[0]) + __uint_as_float(t[1]);
}
template <int CTRL> DI float dppf(float x) { return __builtin_bit_cast(float, __builtin_amdgcn_mov_dpp(__builtin_bit_cast(int, x), CTRL, 0xf, 0xf, true)); }
template <int CTRL> DI float dpp_add(float x) { return x + dppf<CTRL>(x); }
DI float wave_sum(float x) { x = dpp_add<0x121>(x); x = dpp_add<0x122>(x); x = dpp_add<0x124>(x); x = dpp_add<0x128>(x); return xg_sum(x); }
DI float wave_max(float x) {
    x = fmaxf(x, dppf<0x121>(x)); x = fmaxf(x, dppf<0x122>(x)); x = fmaxf(x, dppf<0x124>(x)); x = fmaxf(x, dppf<0x128>(x));
    auto s = __builtin_amdgcn_permlane16_swap(__float_as_uint(x), __float_as_uint(x), false, false); x = fmaxf(__uint_as_float(s[0]), __uint_as_float(s[1]));
    auto t = __builtin_amdgcn_permlane32_swap(__float_as_uint(x), __float_as_uint(x), false, false); return fmaxf(__uint_as_float(t[0]), __uint_as_float(t[1]));
}
DI int wave_argmax(float v, float& best) {
    best = wave_max(v); const unsigned long long m = __ballot(v == best); return m ? __builtin_ctzll(m) : 0;
}
DI float lo32_bcast(float x) { auto t = __builtin_amdgcn_permlane32_swap(__float_as_uint(x), __float_as_uint(x), false, false); return __uint_as_float(t[0]); }
#define STAMP_DECL
#define STAMP(k, i)
#define STAMP_FLUSH(k)
#define BAR_LDS() asm volatile("s_waitcnt lgkmcnt(0)\n\ts_barrier" ::: "memory")
#define BAR_ALL() asm volatile("s_waitcnt vmcnt(0) lgkmcnt(0)\n\ts_barrier" ::: "memory")

DI int blk_group(int b) { return (b & 7) + 8 * (b >> 6); }
DI int blk_member(int b) { return (b >> 3) & 7; }

template <class LD> DI void gemm45(f32x4 (&acc)[5], const bf16x8 (&xf)[9], LD ld) {
    bf16x8 cur[5], nxt[5];
#pragma unroll
    for (int ct = 0; ct < 5; ++ct) cur[ct] = ld(0, ct);
#pragma unroll
    for (int s = 0; s < 9; ++s) {
        if (s < 8) {
#pragma unroll
            for (int ct = 0; ct < 5; ++ct) nxt[ct] = ld(s + 1, ct);
        }
        SBAR();
#pragma unroll
        for (int ct = 0; ct < 5; ++ct) acc[ct] = MFMA16(cur[ct], xf[s], acc[ct]);
        SBAR();
#pragma unroll
        for (int ct = 0; ct < 5; ++ct) cur[ct] = nxt[ct];
    }
}
DI void khalf_reduce(LAS f32x4* ex, const f32x4 (&acc)[5], f32x4 (&out)[3], int rt, int kh, int lane) {
    if (kh == 0) { ex[(rt * 5 + 3) * 64 + lane] = acc[3]; ex[(rt * 5 + 4) * 64 + lane] = acc[4]; }
    else { ex[(rt * 5 + 0) * 64 + lane] = acc[0]; ex[(rt * 5 + 1) * 64 + lane] = acc[1]; ex[(rt * 5 + 2) * 64 + lane] = acc[2]; }
    BAR_LDS();
    if (kh == 0) { out[0] = acc[0] + ex[(rt * 5 + 0) * 64 + lane]; out[1] = acc[1] + ex[(rt * 5 + 1) * 64 + lane]; out[2] = acc[2] + ex[(rt * 5 + 2) * 64 + lane]; }
    else { out[0] = acc[3] + ex[(rt * 5 + 3) * 64 + lane]; out[1] = acc[4] + ex[(rt * 5 + 4) * 64 + lane]; out[2] = f32x4{0, 0, 0, 0}; }
}
typedef __amdgpu_buffer_rsrc_t rsrc_t;
DI rsrc_t mkrs(const void* p, unsigned bytes) { return __builtin_amdgcn_make_buffer_rsrc((void*)p, 0, bytes, 0x00020000); }
constexpr unsigned OOB = 0x80000000u;
DI float dot2bf(unsigned a, unsigned b, float c) { return __builtin_amdgcn_fdot2_f32_bf16(__builtin_bit_cast(bf16x2, a), __builtin_bit_cast(bf16x2, b), c, false); }
struct QkvOff { unsigned d[3][2]; unsigned v; float scl4; };
DI QkvOff qkv_offsets(size_t qf, size_t kvp, int b, int m, int T, int kh, int gq) {
    QkvOff o; const int r = T & 31; const unsigned kt = (unsigned)kvp + (T >> 5) * TILE_B;
#pragma unroll
    for (int i = 0; i < 3; ++i)
#pragma unroll
        for (int e = 0; e < 2; ++e) { const int ct = kh == 0 ? i : 3 + i, n = 16 * ct + 4 * gq + 2 * e, p = (n * 57) >> 10, d = n - 18 * p;
            const unsigned qo = (unsigned)qf + (b * 8 + p * 2 + ((T & 63) >> 5)) * 1152 + (d < 16 ? ((d >> 3) * 32 + r) * 16 + (d & 7) * 2 : 1024 + r * 4);
            unsigned ko = OOB;
            if (m < 2) ko = kt + (m * 32 + r) * 16 + (gq - 2) * 8 + e * 4; else if (m == 4 && gq == 3 && e == 1) ko = kt + K2_OFF + r * 4;
            o.d[i][e] = ct > 4 ? OOB : (n < 72 ? qo : ko); }
    o.v = OOB;
    if (kh == 1 && gq >= 2) { const unsigned vb = kt + V_OFF + (r >> 3) * VSUB + (r & 7) * 2;
        if (m == 2 || m == 3) o.v = vb + (8 * (m - 2) + 4 * (gq - 2)) * 16; else if (m == 5 && gq == 3) o.v = vb + 14 * 16; }
    o.scl4 = (kh == 1 && gq >= 2) ? 1.f : QSCALE;
    return o;
}
#define PIN_QKV(o) do { PIN(o.d[0][0]); PIN(o.d[0][1]); PIN(o.d[1][0]); PIN(o.d[1][1]); PIN(o.d[2][0]); PIN(o.d[2][1]); PIN(o.v); PIN(o.scl4); } while (0)
DI void store_qkv3(rsrc_t rs, const QkvOff& o, size_t kvp, int m, int g, int tid, const f32x4 (&a)[3]) {
#pragma unroll
    for (int i = 0; i < 3; ++i) { const float sc = i == 1 ? o.scl4 : QSCALE;
        __builtin_amdgcn_raw_buffer_store_b32(pk2(a[i][0] * sc, a[i][1] * sc), rs, o.d[i][0], 0, 0);
        __builtin_amdgcn_raw_buffer_store_b32(pk2(a[i][2] * sc, a[i][3] * sc), rs, o.d[i][1], 0, 0); }
    if (m == 2 || m == 3 || m == 5) { const unsigned lo = pk2(a[1][0], a[1][1]), hi = pk2(a[1][2], a[1][3]);
        if (m != 5) { __builtin_amdgcn_raw_buffer_store_b16((u16)lo, rs, o.v, 0, 0); __builtin_amdgcn_raw_buffer_store_b16((u16)(lo >> 16), rs, o.v + 16, 0, 0); }
        __builtin_amdgcn_raw_buffer_store_b16((u16)hi, rs, o.v + 32, 0, 0); __builtin_amdgcn_raw_buffer_store_b16((u16)(hi >> 16), rs, o.v + 48, 0, 0); }
    if (m >= 6) { const unsigned v = m == 6 ? 0x3F803F80u : 0u; u32x4 c = {v, v, v, v};
        __builtin_amdgcn_raw_buffer_store_b128(c, rs, tid < 8 ? (unsigned)kvp + (2 * g + (tid >> 2)) * TILE_B + V_OFF + (tid & 3) * VSUB + (m == 6 ? 18 : 19) * 16 : OOB, 0, 0); }
}
DI void ln_sums(const u32x4 (&xa)[9], float& sum, float& sq) {
    float s0 = 0.f, s1 = 0.f, q0 = 0.f, q1 = 0.f;
#pragma unroll
    for (int s = 0; s < 9; ++s) { s0 = dot2bf(xa[s][0], 0x3F803F80u, s0); s1 = dot2bf(xa[s][1], 0x3F803F80u, s1); s0 = dot2bf(xa[s][2], 0x3F803F80u, s0); s1 = dot2bf(xa[s][3], 0x3F803F80u, s1);
        q0 = dot2bf(xa[s][0], xa[s][0], q0); q1 = dot2bf(xa[s][1], xa[s][1], q1); q0 = dot2bf(xa[s][2], xa[s][2], q0); q1 = dot2bf(xa[s][3], xa[s][3], q1); }
    sum = xg_sum(s0 + s1); sq = xg_sum(q0 + q1);
}
DI void ln_apply(const u32x4 (&xa)[9], float mu, float sc, bf16x8 (&xf)[9]) {
    const float nb = -mu * sc;
#pragma unroll
    for (int s = 0; s < 9; ++s) { u32x4 o;
#pragma unroll
        for (int j = 0; j < 4; ++j) o[j] = pk2(__builtin_fmaf(bflo(xa[s][j]), sc, nb), __builtin_fmaf(bfhi(xa[s][j]), sc, nb));
        xf[s] = __builtin_bit_cast(bf16x8, o); }
}

constexpr int WROW = 144, WIMG = 576 * WROW, KVIMG = 576 * 16;
constexpr int QKV_LDS = WIMG + KVIMG + 2048 + 20480;
__global__ __launch_bounds__(512, 2) void k_qkv(Params p) {
    extern __shared__ __attribute__((aligned(16))) char lds[];
    const int tid = threadIdx.x, lane = tid & 63, wave = __builtin_amdgcn_readfirstlane(tid >> 6), rt = wave & 3, kh = wave >> 2;
    const int b = blockIdx.x, g = blk_group(b), m = blk_member(b), r16 = lane & 15, gq = lane >> 4;
    const unsigned lds0 = (unsigned)(uintptr_t)lds;
    STAMP_DECL; STAMP(0, 0);
    const int T = g * 64 + rt * 16 + r16;
    const u16* xsrc = p.x + (size_t)T * D + kh * 288 + gq * 8;
    u32x4 xa[9];
    asm volatile("global_load_dwordx4 %0, %9, off\n\tglobal_load_dwordx4 %1, %9, off offset:64\n\tglobal_load_dwordx4 %2, %9, off offset:128\n\t"
                 "global_load_dwordx4 %3, %9, off offset:192\n\tglobal_load_dwordx4 %4, %9, off offset:256\n\tglobal_load_dwordx4 %5, %9, off offset:320\n\t"
                 "global_load_dwordx4 %6, %9, off offset:384\n\tglobal_load_dwordx4 %7, %9, off offset:448\n\tglobal_load_dwordx4 %8, %9, off offset:512"
                 : "=&v"(xa[0]), "=&v"(xa[1]), "=&v"(xa[2]), "=&v"(xa[3]), "=&v"(xa[4]), "=&v"(xa[5]), "=&v"(xa[6]), "=&v"(xa[7]), "=&v"(xa[8]) : "v"(xsrc) : "memory");
    { const char* qsrc = (const char*)p.wq + 144 * m;
      int prow = (tid * 7282) >> 16, pc = tid - prow * 9;
#pragma unroll
      for (int i = 0; i < 11; ++i) { if (i < 10 || wave == 0) {
          const int k = (prow & ~12) | ((prow & 4) << 1) | ((prow & 8) >> 1);
          glds16s(qsrc, k * 1152 + 16 * pc, __builtin_amdgcn_readfirstlane(lds0 + (i * 512 + wave * 64) * 16)); }
          const bool wrap = pc >= 1; pc = wrap ? pc - 1 : pc + 8; prow += wrap ? 57 : 56; }
      const char* kvsrc = (const char*)((m == 2 || m == 3 || m == 5) ? p.wv : p.wk) + ((m == 1 || m == 3) ? 16 : (m == 4 || m == 5) ? 20 : 0);
      { const int pr = tid, k = (pr & ~12) | ((pr & 4) << 1) | ((pr & 8) >> 1); glds16s(kvsrc, k * 36, __builtin_amdgcn_readfirstlane(lds0 + WIMG + wave * 1024)); }
      if (wave == 1) { const int pr = 512 + lane, k = (pr & ~12) | ((pr & 4) << 1) | ((pr & 8) >> 1); glds16s(kvsrc, k * 36, __builtin_amdgcn_readfirstlane(lds0 + WIMG + 8192)); } }
    STAMP(0, 1);
    const rsrc_t rws = mkrs(p.ws, WS_END);
    QkvOff qo = qkv_offsets(WS_QF, WS_KV, b, m, T, kh, gq); PIN_QKV(qo);
    asm volatile("s_waitcnt vmcnt(11)" : "+v"(xa[0]), "+v"(xa[1]), "+v"(xa[2]), "+v"(xa[3]), "+v"(xa[4]), "+v"(xa[5]), "+v"(xa[6]), "+v"(xa[7]), "+v"(xa[8]) :: "memory");
    float sum, sq; ln_sums(xa, sum, sq);
    LAS f32x2* st = (LAS f32x2*)(lds + WIMG + KVIMG);
    if (gq == 0) st[wave * 16 + r16] = f32x2{sum, sq};
    STAMP(0, 2);
    BAR_ALL();
    STAMP(0, 3);
    unsigned wog[8];
    const int wf = g + 32 * (wave < 3 ? wave : 0), wfc = wf < 90 ? wf : 89, fS = wfc / 5, fct = wfc - fS * 5;
    { const int c = 4 * fS + gq, mm = c / 9, cc = c - mm * 9, n = 16 * fct + r16, nc = n < 72 ? n : 71;
#pragma unroll
      for (int j = 0; j < 8; ++j) { const int head = 4 * mm + (cc < 8 ? (cc >> 1) : (j >> 1)), d = cc < 8 ? (j < 4 ? 4 * (cc & 1) + j : 8 + 4 * (cc & 1) + j - 4) : 16 + (j & 1);
          wog[j] = p.wo[(head * 18 + d) * 576 + 72 * m + nc]; } }
    { const f32x2 o = st[(wave ^ 4) * 16 + r16]; sum += o[0]; sq += o[1]; }
    const float mu = sum * (1.f / D), var = fmaxf(sq * (1.f / D) - mu * mu, 0.f), rs = rsqrtf(var + 1e-5f);
    bf16x8 xf[9]; ln_apply(xa, mu, rs, xf);
    STAMP(0, 4);
    const int prow0 = 16 * (gq >> 1) + 4 * (gq & 1) + (r16 >> 2);
    LAS const char* wb = (LAS const char*)lds + (kh * 288 + prow0) * WROW + (r16 & 3) * 8;
    LAS const char* wb4 = (r16 & 3) < 2 ? wb + 128 : (LAS const char*)lds + WIMG + (kh * 288 + prow0) * 16 + ((r16 & 3) - 2) * 8;
    const int st4 = (r16 & 3) < 2 ? WROW : 16;
    auto ldw = [&](int s, int ct) { const s16x4 lo = ct < 4 ? trread(wb + s * 32 * WROW + ct * 32) : trread(wb4 + s * 32 * st4),
                                                hi = ct < 4 ? trread(wb + s * 32 * WROW + 8 * WROW + ct * 32) : trread(wb4 + (s * 32 + 8) * st4);
        return __builtin_bit_cast(bf16x8, __builtin_shufflevector(lo, hi, 0, 1, 2, 3, 4, 5, 6, 7)); };
    f32x4 acc[5] = {}, o3[3];
    gemm45(acc, xf, ldw);
    STAMP(0, 5);
    khalf_reduce((LAS f32x4*)(lds + WIMG + KVIMG + 2048), acc, o3, rt, kh, lane);
    store_qkv3(rws, qo, WS_KV, m, g, tid, o3);
    if (wave < 3 && wf < 90) {
        u32x4 o = {wog[0] | (wog[1] << 16), wog[2] | (wog[3] << 16), wog[4] | (wog[5] << 16), wog[6] | (wog[7] << 16)};
        if (16 * fct + r16 >= 72) o = u32x4{0, 0, 0, 0};
        __builtin_amdgcn_raw_buffer_store_b128(o, rws, (unsigned)WS_WOF + ((m * 90 + wf) * 64 + lane) * 16, 0, 0);
        LAS const char* a0 = fct < 4 ? (LAS const char*)lds + (fS * 32 + prow0) * WROW + (r16 & 3) * 8 + fct * 32
                                     : ((r16 & 3) < 2 ? (LAS const char*)lds + (fS * 32 + prow0) * WROW + (r16 & 3) * 8 + 128 : (LAS const char*)lds + WIMG + (fS * 32 + prow0) * 16 + ((r16 & 3) - 2) * 8);
        const int stp = (fct < 4 || (r16 & 3) < 2) ? WROW : 16;
        const s16x4 lo = trread(a0), hi = trread(a0 + 8 * stp);
        __builtin_amdgcn_raw_buffer_store_b128(__builtin_bit_cast(u32x4, __builtin_shufflevector(lo, hi, 0, 1, 2, 3, 4, 5, 6, 7)), rws, (unsigned)WS_WQF + ((m * 90 + wf) * 64 + lane) * 16, 0, 0);
    }
    if (wave >= 4) __builtin_amdgcn_raw_buffer_store_b128(u32x4{0, 0, 0, 0}, rws, b * 4096 + (tid - 256) * 16, 0, 0);
    STAMP(0, 6);
}

struct FragQ { bf16x8 a1, a2, b1, b2; };
DI void lds_sync(LAS unsigned* cnt, unsigned target, int lane) {
    asm volatile("s_waitcnt lgkmcnt(0)" ::: "memory");
    if (lane == 0) __hip_atomic_fetch_add(cnt, 1u, __ATOMIC_RELAXED, __HIP_MEMORY_SCOPE_WORKGROUP);
    while (__builtin_amdgcn_readfirstlane(__hip_atomic_load(cnt, __ATOMIC_RELAXED, __HIP_MEMORY_SCOPE_WORKGROUP)) < target) __builtin_amdgcn_s_sleep(1);
    asm volatile("" ::: "memory");
}
template <class PF>
DI void attn_pass(LAS const char* lds, const char* kvg, unsigned lds0, int wave, int lane, const FragQ& q, f32x16& oa, f32x16& ob, LAS unsigned* cnt, PF prefetch) {
    const int r = lane & 31, h = lane >> 5, kh = wave >> 2;
    const int dsel = r < 19 ? r : 19;
    LAS const char* kp = lds + kh * TILE_B + lane * 16;
    LAS const char* k2p = lds + kh * TILE_B + K2_OFF + r * 4;
    LAS const char* vp = lds + kh * TILE_B + V_OFF + h * VSUB + dsel * 16;
    const f32x16 zero = {};
#define LDK(i) (*(LAS const bf16x8*)(kp + (i) * 2 * TILE_B))
#define LDK2(i) (*(LAS const unsigned*)(k2p + (i) * 2 * TILE_B))
#define LDV(i, s) (*(LAS const bf16x8*)(vp + (i) * 2 * TILE_B + (s) * 2 * VSUB))
    bf16x8 k1 = LDK(0); u32x4 k2u = {LDK2(0), 0, 0, 0};
    f32x16 sa0, sb0, sa1, sb1;
    sa0 = MFMA32(k1, q.a1, zero); sa0 = MFMA32(__builtin_bit_cast(bf16x8, k2u), q.a2, sa0);
    sb0 = MFMA32(k1, q.b1, zero); sb0 = MFMA32(__builtin_bit_cast(bf16x8, k2u), q.b2, sb0);
    k1 = LDK(1); k2u[0] = LDK2(1);
    bf16x8 v0 = LDV(0, 0), v1 = LDV(0, 1), v1p;
    { u32x4 z = {0, 0, 0, 0}; v1p = __builtin_bit_cast(bf16x8, z); }
    sb1 = f32x16{};
    if (kh == 1) __builtin_amdgcn_s_setprio(1);
    const unsigned voff = wave * 1024 + lane * 16;
#define DMA(c) do { if ((c) >= 0 && (c) < 19) glds16s(kvg + (c) * 8192, voff, __builtin_amdgcn_readfirstlane(lds0 + wave * 1024) + (c) * 8192); } while (0)
#define EX4(S_, j) S_[j] = __builtin_amdgcn_exp2f(S_[j]); S_[j + 1] = __builtin_amdgcn_exp2f(S_[j + 1]); S_[j + 2] = __builtin_amdgcn_exp2f(S_[j + 2]); S_[j + 3] = __builtin_amdgcn_exp2f(S_[j + 3]);
#define STEP(CA, CB, NA, NB, i, C0) do { \
        ob = pv(v1p, hi8(NB), ob); SBAR(); DMA(C0); EX4(CA, 0) PIN(CA); SBAR(); \
        NA = MFMA32(k1, q.a1, zero); SBAR(); DMA((C0) < 0 ? -1 : (C0) + 1); EX4(CA, 4) PIN(CA); SBAR(); \
        NA = MFMA32(__builtin_bit_cast(bf16x8, k2u), q.a2, NA); SBAR(); DMA((C0) < 0 ? -1 : (C0) + 2); EX4(CA, 8) PIN(CA); SBAR(); \
        oa = pv(v0, lo8(CA), oa); SBAR(); DMA((C0) < 0 ? -1 : (C0) + 3); EX4(CA, 12) PIN(CA); SBAR(); \
        NB = MFMA32(k1, q.b1, zero); SBAR(); k1 = LDK((i) + 2); DMA((C0) < 0 ? -1 : (C0) + 4); SBAR(); EX4(CB, 0) PIN(CB); SBAR(); \
        oa = pv(v1, hi8(CA), oa); SBAR(); DMA((C0) < 0 ? -1 : (C0) + 5); EX4(CB, 4) PIN(CB); SBAR(); \
        NB = MFMA32(__builtin_bit_cast(bf16x8, k2u), q.b2, NB); SBAR(); k2u[0] = LDK2((i) + 2); DMA((C0) < 0 ? -1 : (C0) + 6); SBAR(); EX4(CB, 8) PIN(CB); SBAR(); \
        ob = pv(v0, lo8(CB), ob); SBAR(); DMA((C0) < 0 ? -1 : (C0) + 7); EX4(CB, 12) PIN(CB); SBAR(); \
        v1p = v1; v0 = LDV((i) + 1, 0); v1 = LDV((i) + 1, 1); SBAR(); \
    } while (0)
    STEP(sa0, sb0, sa1, sb1, 0, 5);
    STEP(sa1, sb1, sa0, sb0, 1, 13);
    kp += 4 * TILE_B; k2p += 4 * TILE_B; vp += 4 * TILE_B;
    int i = 2;
    for (int seg = 0; seg < 4; ++seg) {
        const int end = seg == 0 ? 6 : seg == 1 ? 8 : seg == 2 ? 22 : 32;
        for (; i < end; i += 2) {
            STEP(sa0, sb0, sa1, sb1, 0, -1);
            STEP(sa1, sb1, sa0, sb0, 1, -1);
            kp += 4 * TILE_B; k2p += 4 * TILE_B; vp += 4 * TILE_B;
        }
        if (seg == 0) {
            asm volatile("s_waitcnt vmcnt(0)" ::: "memory");
            if (cnt) lds_sync(cnt, 16u, lane); else asm volatile("s_barrier" ::: "memory"); }
        else if (seg == 1) prefetch();
        else if (seg == 2) __builtin_amdgcn_s_setprio(0);
    }
    ob = pv(v1p, hi8(sb1), ob);
#undef STEP
#undef DMA
#undef LDK
#undef LDK2
#undef LDV
}
template <int MODE>
DI void attn_slow(LAS const char* lds, int t0, int dt, int n, int lane, const FragQ& q, float& ma, float& mb, f32x16& oa, f32x16& ob) {
    const int r = lane & 31, h = lane >> 5, dsel = r < 19 ? r : 19; const f32x16 zero = {};
    if (MODE == 0) { ma = -INFINITY; mb = -INFINITY; }
    for (int i = 0; i < n; ++i) { LAS const char* t = lds + (t0 + i * dt) * TILE_B;
        const bf16x8 k1 = *(LAS const bf16x8*)(t + lane * 16); u32x4 k2u = {*(LAS const unsigned*)(t + K2_OFF + r * 4), 0, 0, 0};
        f32x16 sa = MFMA32(k1, q.a1, zero); sa = MFMA32(__builtin_bit_cast(bf16x8, k2u), q.a2, sa);
        if (MODE == 2) { const bf16x8 v0 = *(LAS const bf16x8*)(t + V_OFF + h * VSUB + dsel * 16), v1 = *(LAS const bf16x8*)(t + V_OFF + (2 + h) * VSUB + dsel * 16);
            for (int j = 0; j < 16; ++j) sa[j] = __builtin_amdgcn_exp2f(sa[j]);
            oa = pv(v0, lo8(sa), oa); oa = pv(v1, hi8(sa), oa); continue; }
        f32x16 sb = MFMA32(k1, q.b1, zero); sb = MFMA32(__builtin_bit_cast(bf16x8, k2u), q.b2, sb);
        if (MODE == 0) { for (int j = 0; j < 16; ++j) { ma = fmaxf(ma, sa[j]); mb = fmaxf(mb, sb[j]); } }
        else { const bf16x8 v0 = *(LAS const bf16x8*)(t + V_OFF + h * VSUB + dsel * 16), v1 = *(LAS const bf16x8*)(t + V_OFF + (2 + h) * VSUB + dsel * 16);
            for (int j = 0; j < 16; ++j) { sa[j] = __builtin_amdgcn_exp2f(sa[j] - ma); sb[j] = __builtin_amdgcn_exp2f(sb[j] - mb); }
            oa = pv(v0, lo8(sa), oa); oa = pv(v1, hi8(sa), oa); ob = pv(v0, lo8(sb), ob); ob = pv(v1, hi8(sb), ob); } }
    if (MODE == 0) { ma = fmaxf(ma, __shfl_xor(ma, 32)); mb = fmaxf(mb, __shfl_xor(mb, 32)); }
}
DI bool o_bad(const f32x16& o, int h) {
    bool bad = false;
    if (h == 0) { const float l = o[10]; bad = !(l >= 0x1p-60f && l <= 0x1p59f); }
    for (int j = 0; j < 10; ++j) bad |= !(fabsf(o[j]) < INFINITY);
    return bad;
}
DI void wait_flags8(GAS unsigned* flags, unsigned epoch, int lane) {
    unsigned sp = 0;
    for (;;) { const unsigned f = lane < 8 ? __hip_atomic_load(flags + lane * 64, RLX_AGENT) : epoch; if (__all(f == epoch) || ++sp > (1u << 22)) break; }
}
DI void put_granule(char* ws, unsigned off, unsigned v) { __hip_atomic_store((GAS unsigned long long*)(ws + off), (1ull << 32) | v, RLX_AGENT); }
DI unsigned long long ld_granule(char* ws, unsigned off) { return __hip_atomic_load((GAS unsigned long long*)(ws + off), RLX_AGENT); }
template <int N> DI void get_granules(char* ws, unsigned off, unsigned stride, bool act, unsigned (&v)[N], bool nap) {
    unsigned sp = 0;
    for (;;) { bool ok = true;
#pragma unroll
        for (int k = 0; k < N; ++k) { const unsigned long long x = act ? ld_granule(ws, off + k * stride) : (1ull << 32); v[k] = (unsigned)x; ok &= (x >> 32) != 0; }
        if (__all(ok) || ++sp > (1u << 20)) break;
        if (nap) __builtin_amdgcn_s_sleep(4); }
}

constexpr int ATT_LDS = 160 * 1024;
constexpr int B1_OFF = 32768;
constexpr int SP = KV_B;
constexpr int SP_W = SP, SP_T = SP + 64, SP_XS = SP + 1088, SP_QN = SP + 3392, SP_KVN = SP + 3712, SP_OZ = SP + 3872, SP_PZ = SP + 4192, SP_SCR = SP + 5216;
constexpr int SP_RED = SP + 1088, SP_HS = SP + 5184;
enum { W_ASYNC = 0, W_HSYNC = 1, W_READY = 2, W_SEL = 3, W_OZBAD = 4, W_WSUM = 5, W_OZDONE = 6, W_BAD = 8 };

struct AttnCtx { int tid, lane, wave, r, h, pp, kh, b, g, m, rt, r16, gq, T2; unsigned lds0; };
DI AttnCtx attn_ctx(char* lds) { AttnCtx c; c.tid = threadIdx.x; c.lane = c.tid & 63; c.r = c.lane & 31; c.h = c.lane >> 5; c.wave = __builtin_amdgcn_readfirstlane(c.tid >> 6);
    c.pp = c.wave & 3; c.kh = (c.wave >> 2) & 1; c.b = blockIdx.x; c.g = blk_group(c.b); c.m = blk_member(c.b); c.rt = c.wave & 3; c.r16 = c.lane & 15; c.gq = c.lane >> 4;
    c.T2 = c.g * 64 + c.rt * 16 + c.r16; c.lds0 = (unsigned)(uintptr_t)lds; return c; }
DI void load_q_stage1(const AttnCtx& c, const char* qb, const char* kv, FragQ& q) {
    u32x4 qa1, qb1; unsigned qa2r, qb2r;
    asm volatile("global_load_dwordx4 %0, %4, off\n\tglobal_load_dwordx4 %1, %4, off offset:1152\n\tglobal_load_dword %2, %5, off offset:1024\n\tglobal_load_dword %3, %5, off offset:2176"
                 : "=&v"(qa1), "=&v"(qb1), "=&v"(qa2r), "=&v"(qb2r) : "v"(qb + c.lane * 16), "v"(qb + c.r * 4) : "memory");
#pragma unroll
    for (int i = 0; i < 5; ++i) glds16s(kv + i * 8192, c.wave * 1024 + c.lane * 16, __builtin_amdgcn_readfirstlane(c.lds0 + c.wave * 1024) + i * 8192);
    asm volatile("s_waitcnt vmcnt(0)" : "+v"(qa1), "+v"(qb1), "+v"(qa2r), "+v"(qb2r) :: "memory");
    q.a1 = __builtin_bit_cast(bf16x8, qa1); q.b1 = __builtin_bit_cast(bf16x8, qb1);
    u32x4 t; t[0] = c.h ? 0u : qa2r; t[1] = 0; t[2] = 0; t[3] = 0; q.a2 = __builtin_bit_cast(bf16x8, t); t[0] = c.h ? 0u : qb2r; q.b2 = __builtin_bit_cast(bf16x8, t);
}
DI LAS f32x4* exbuf(char* lds, int w) { return (LAS f32x4*)(lds + w * 3072); }
DI void put_partial(char* lds, const AttnCtx& c, const f32x16& oa, const f32x16& ob) { LAS f32x4* ex = exbuf(lds, c.wave); const f32x16& snd = c.kh ? oa : ob;
    ex[c.lane] = f32x4{snd[0], snd[1], snd[2], snd[3]}; ex[64 + c.lane] = f32x4{snd[4], snd[5], snd[6], snd[7]}; ex[128 + c.lane] = f32x4{snd[8], snd[9], snd[10], snd[11]}; }
DI bool any_bad(char* lds) { LAS const u32x4* f = (LAS const u32x4*)(lds + SP_W + W_BAD * 4); const u32x4 f0 = f[0], f1 = f[1];
    return __builtin_amdgcn_readfirstlane(f0[0] | f0[1] | f0[2] | f0[3] | f1[0] | f1[1] | f1[2] | f1[3]) != 0; }
DI void fallback_restore(const AttnCtx& c, const char* kv, bool loader) {
    BAR_LDS();
    if (loader) {
#pragma unroll
        for (int i = 0; i < 3; ++i) glds16s(kv + i * 8192, c.wave * 1024 + c.lane * 16, __builtin_amdgcn_readfirstlane(c.lds0 + c.wave * 1024) + i * 8192); }
    BAR_ALL();
}
DI void fallback_pass(char* lds, const AttnCtx& c, const FragQ& q, f32x16& oa, f32x16& ob) {
    LAS float* scr = (LAS float*)(lds + SP_SCR);
    float ma, mb; attn_slow<0>((LAS const char*)lds, c.kh, 2, 32, c.lane, q, ma, mb, oa, ob);
    if (c.h == 0) { scr[(c.wave * 2) * 32 + c.r] = ma; scr[(c.wave * 2 + 1) * 32 + c.r] = mb; }
    BAR_LDS();
    ma = fmaxf(ma, scr[((c.wave ^ 4) * 2) * 32 + c.r]); mb = fmaxf(mb, scr[((c.wave ^ 4) * 2 + 1) * 32 + c.r]);
    oa = f32x16{}; ob = f32x16{};
    attn_slow<1>((LAS const char*)lds, c.kh, 2, 32, c.lane, q, ma, mb, oa, ob);
    BAR_LDS();
    put_partial(lds, c, oa, ob);
    BAR_LDS();
}
DI void finish_o(char* lds, const AttnCtx& c, const f32x16& oa, const f32x16& ob, f32x4& a, f32x4& e, f32x2& z) {
    LAS const f32x4* rx = exbuf(lds, c.wave ^ 4); const f32x16& own = c.kh ? ob : oa;
    a = rx[c.lane] + f32x4{own[0], own[1], own[2], own[3]}; e = rx[64 + c.lane] + f32x4{own[4], own[5], own[6], own[7]};
    const f32x4 t = rx[128 + c.lane] + f32x4{own[8], own[9], own[10], own[11]};
    const float il = __builtin_amdgcn_rcpf(lo32_bcast(t[2]));
    a *= il; e *= il; z = f32x2{t[0] * il, t[1] * il};
}
DI void publish_o(rsrc_t rws, const AttnCtx& c, const f32x4& a, const f32x4& e, const f32x2& z) {
    const unsigned xo = (unsigned)WS_XO + c.g * XO_G;
    u32x4 v = {pk2(a[0], a[1]), pk2(a[2], a[3]), pk2(e[0], e[1]), pk2(e[2], e[3])};
    __builtin_amdgcn_raw_buffer_store_b128(v, rws, xo + ((9 * c.m + 2 * c.pp + c.h) * 64 + 32 * c.kh + c.r) * 16, 0, 16);
    __builtin_amdgcn_raw_buffer_store_b32(pk2(z[0], z[1]), rws, c.h == 0 ? xo + ((9 * c.m + 8) * 64 + 32 * c.kh + c.r) * 16 + c.pp * 4 : OOB, 0, 16);
}
DI void stage_frags(const AttnCtx& c, const char* src, int w, int nw) {
    for (int f = w; f < 90; f += nw) glds16s(src + f * 1024, c.lane * 16, __builtin_amdgcn_readfirstlane(c.lds0 + B1_OFF + f * 1024));
}

__global__ __launch_bounds__(512, 2) void k_attn0(Params p) {
    extern __shared__ __attribute__((aligned(16))) char lds[];
    const AttnCtx c = attn_ctx(lds);
    const int tid = c.tid, lane = c.lane, wave = c.wave, kh = c.kh, m = c.m, g = c.g, rt = c.rt, r16 = c.r16, gq = c.gq, T2 = c.T2;
    STAMP_DECL; STAMP(1, 0);
    const char* kv = p.ws + WS_KV;
    FragQ q; load_q_stage1(c, p.ws + WS_QF + ((size_t)c.b * 8 + c.pp * 2) * 1152, kv, q);
    asm volatile("s_barrier" ::: "memory");
    STAMP(1, 1);
    f32x16 oa = {}, ob = {};
    attn_pass((LAS const char*)lds, kv, c.lds0, wave, lane, q, oa, ob, nullptr, [&]() {
        if ((c.b >> 3) < 18) { const int fc = (c.b & 7) * 18 + (c.b >> 3); const unsigned dst = __builtin_amdgcn_readfirstlane(c.lds0 + SP + 4096 + wave * 256);
            const int row = wave * 64 + lane;
            glds4s((const char*)p.wg + fc * 64, row * (F * 2), dst); glds4s((const char*)p.wu + fc * 64, row * (F * 2), dst);
            if (wave == 0) { glds4s((const char*)p.wg + fc * 64, (512 + lane) * (F * 2), dst); glds4s((const char*)p.wu + fc * 64, (512 + lane) * (F * 2), dst); }
            glds4s((const char*)p.wd + (size_t)fc * 32 * D * 2, (tid < 288 ? tid : 0) * 128, dst); } });
    STAMP(1, 2);
    { const bool bad = __any(o_bad(oa, c.h) || o_bad(ob, c.h)); if (lane == 0) ((LAS unsigned*)(lds + SP_W))[W_BAD + wave] = bad; put_partial(lds, c, oa, ob); }
    BAR_LDS();
    if (__builtin_expect(any_bad(lds), 0)) { fallback_restore(c, kv, true); fallback_pass(lds, c, q, oa, ob); }
    STAMP(1, 3);
    const rsrc_t rws = mkrs(p.ws, WS_END);
    { f32x4 a, e; f32x2 z; finish_o(lds, c, oa, ob, a, e, z); publish_o(rws, c, a, e, z); }
    STAMP(1, 4);
    BAR_ALL();
    GAS unsigned* flags = (GAS unsigned*)(p.ws + WS_FLAG) + (size_t)g * 8 * 64;
    if (tid == 0) __hip_atomic_store(flags + m * 64, 1u, RLX_AGENT);
    if (wave != 0) stage_frags(c, p.ws + WS_WOF + (size_t)m * FRAGS_B, wave - 1, 7);
    const unsigned xo = (unsigned)WS_XO + g * XO_G, xx = (unsigned)WS_XX + g * XO_G;
    const unsigned aoff = ((4 * 9 * kh + gq) * 64 + rt * 16 + r16) * 16;
    unsigned coff[3], xxo[3]; u32x2 resb[3]; u32x4 wrv[9];
#pragma unroll
    for (int i = 0; i < 3; ++i) { const int ct = kh == 0 ? i : 3 + i, n = 16 * ct + 4 * gq;
        resb[i] = *(const u32x2*)(p.x + (size_t)T2 * D + 72 * m + (n < 72 ? n : 68));
        coff[i] = n < 72 ? T2 * D + 72 * m + n : OOB / 4; PIN(coff[i]);
        xxo[i] = n < 72 ? xx + ((9 * m + 2 * ct + (gq >> 1)) * 64 + rt * 16 + r16) * 16 + (gq & 1) * 8 : OOB; PIN(xxo[i]); }
#pragma unroll
    for (int s = 0; s < 9; ++s) wrv[s] = *(const u32x4*)(p.wr + kh * 288 + gq * 8 + s * 32);
    QkvOff qo = qkv_offsets(WS_QF + QF_B, WS_KV + KVP_B, c.b, m, T2, kh, gq); PIN_QKV(qo);
    if (wave == 0) wait_flags8(flags, 1u, lane);
    STAMP(1, 5);
    asm volatile("s_barrier" ::: "memory");
    bf16x8 af[9];
#pragma unroll
    for (int s = 0; s < 9; ++s) af[s] = __builtin_bit_cast(bf16x8, __builtin_amdgcn_raw_buffer_load_b128(rws, aoff, xo + s * 4096, 16));
    BAR_ALL();
    STAMP(1, 6);
    LAS const char* bb = (LAS const char*)lds + B1_OFF + kh * 45 * 1024 + lane * 16;
    auto ldb = [&](int s, int ct) { return *(LAS const bf16x8*)(bb + (s * 5 + ct) * 1024); };
    f32x4 x1[3];
    { f32x4 acc[5] = {}; gemm45(acc, af, ldb); STAMP(1, 7); khalf_reduce((LAS f32x4*)lds, acc, x1, rt, kh, lane); }
#pragma unroll
    for (int i = 0; i < 3; ++i) x1[i] += f32x4{bflo(resb[i][0]), bfhi(resb[i][0]), bflo(resb[i][1]), bfhi(resb[i][1])};
#pragma unroll
    for (int i = 0; i < 3; ++i) { u32x2 w = {pk2(x1[i][0], x1[i][1]), pk2(x1[i][2], x1[i][3])}; __builtin_amdgcn_raw_buffer_store_b64(w, rws, xxo[i], 0, 16); }
    BAR_ALL();
    if (tid == 0) __hip_atomic_store(flags + m * 64 + 32, 1u, RLX_AGENT);
    if (wave != 0) stage_frags(c, p.ws + WS_WQF + (size_t)m * FRAGS_B, wave - 1, 7); else wait_flags8(flags + 32, 1u, lane);
    STAMP(1, 8);
    asm volatile("s_barrier" ::: "memory");
    u32x4 xa[9];
#pragma unroll
    for (int s = 0; s < 9; ++s) xa[s] = __builtin_amdgcn_raw_buffer_load_b128(rws, aoff, xx + s * 4096, 16);
    float wsum = 0.f;
#pragma unroll
    for (int s = 0; s < 9; ++s)
#pragma unroll
        for (int j = 0; j < 4; ++j) wsum = dot2bf(wrv[s][j], 0x3F803F80u, wsum);
    float sum, sq, dot = 0.f; ln_sums(xa, sum, sq);
#pragma unroll
    for (int s = 0; s < 9; ++s)
#pragma unroll
        for (int j = 0; j < 4; ++j) dot = dot2bf(xa[s][j], wrv[s][j], dot);
    dot = xg_sum(dot); wsum = xg_sum(wsum);
    LAS f32x4* st = (LAS f32x4*)(lds + 20480);
    if (gq == 0) st[wave * 16 + r16] = f32x4{sum, sq, dot, wsum};
    BAR_ALL();
    STAMP(1, 9);
    { const f32x4 o = st[(wave ^ 4) * 16 + r16]; sum += o[0]; sq += o[1]; dot += o[2]; wsum += o[3]; }
    const float mu = sum * (1.f / D), var = fmaxf(sq * (1.f / D) - mu * mu, 0.f), rs = rsqrtf(var + 1e-5f);
    const float sc = (1.f + rs) * rsqrtf(var * (1.f + rs) * (1.f + rs) + 1e-5f);
    bf16x8 xf[9]; ln_apply(xa, mu, sc, xf);
    LAS float* rsc = (LAS float*)(lds + 24576);
    if (kh == 0 && gq == 0) rsc[rt * 16 + r16] = rs * (dot - mu * wsum);
#pragma unroll
    for (int i = 0; i < 3; ++i) { const f32x4 xn = (x1[i] - mu) * rs;
        __builtin_amdgcn_raw_buffer_store_b128(__builtin_bit_cast(u32x4, x1[i] + xn), rws, coff[i] * 4, (unsigned)WS_X2, 0);
        u32x2 w = {pk2(xn[0], xn[1]), pk2(xn[2], xn[3])}; __builtin_amdgcn_raw_buffer_store_b64(w, rws, coff[i] * 2, (unsigned)WS_XN, 0); }
    f32x4 o3[3];
    { f32x4 acc[5] = {}; gemm45(acc, xf, ldb); STAMP(1, 10); khalf_reduce((LAS f32x4*)lds, acc, o3, rt, kh, lane); }
    store_qkv3(rws, qo, WS_KV + KVP_B, m, g, tid, o3);
    if (m == 0 && wave == 7) { float best; const int i = wave_argmax(rsc[lane], best);
        if (lane == 0) { u32x2 v = {__float_as_uint(best), (unsigned)(g * 64 + i)}; __builtin_amdgcn_raw_buffer_store_b64(v, rws, (unsigned)WS_CAND0 + g * 8, 0, 0); } }
    STAMP(1, 11);
}

struct FfnW { u32x4 g[9], u[9], d[11]; };
DI void ffn_load(FfnW& w, const Params& p, int c, int t, int hw, int lane) {
    const rsrc_t rg_ = mkrs(p.wg, D * F * 2), ru_ = mkrs(p.wu, D * F * 2), rd_ = mkrs(p.wd, D * F * 2);
    const unsigned vo = (t >> 2) * (F * 2) + c * 64 + (t & 3) * 16;
#pragma unroll
    for (int i = 0; i < 9; ++i) { w.g[i] = __builtin_amdgcn_raw_buffer_load_b128(rg_, vo, i * 64 * F * 2, 0); w.u[i] = __builtin_amdgcn_raw_buffer_load_b128(ru_, vo, i * 64 * F * 2, 0); }
    const int l = lane < 54 ? lane : 0, rg = (l * 57) >> 10, cp = l - rg * 18;
    const unsigned vd = (c * 32 + rg) * (D * 2) + hw * 288 + cp * 16;
#pragma unroll
    for (int i = 0; i < 11; ++i) w.d[i] = __builtin_amdgcn_raw_buffer_load_b128(rd_, (i == 10 && rg == 2) ? OOB : vd, i * 3 * D * 2, 0);
}
DI void ffn_partial(const FfnW& w, const unsigned (&xv)[9], char* lds, char* ws, rsrc_t rws, int use, int c, int t, int hw, int lane, unsigned hs0) {
    LAS float* red = (LAS float*)(lds + SP_RED); LAS unsigned* hv_ = (LAS unsigned*)(lds + SP_HS) + hw * 36;
    LAS unsigned* hcnt = (LAS unsigned*)(lds + SP_W) + W_HSYNC;
    float ag[8] = {}, au[8] = {};
#pragma unroll
    for (int i = 0; i < 9; ++i) { const unsigned x = xv[i], xh = x << 16;
#pragma unroll
        for (int j = 0; j < 4; ++j) { ag[2 * j] = dot2bf(w.g[i][j], x, ag[2 * j]); ag[2 * j + 1] = dot2bf(w.g[i][j], xh, ag[2 * j + 1]);
                                      au[2 * j] = dot2bf(w.u[i][j], x, au[2 * j]); au[2 * j + 1] = dot2bf(w.u[i][j], xh, au[2 * j + 1]); } }
#pragma unroll
    for (int j = 0; j < 8; ++j) { ag[j] = dpp_add<0x128>(dpp_add<0x124>(ag[j])); au[j] = dpp_add<0x128>(dpp_add<0x124>(au[j])); }
    if ((lane & 12) == 0) { LAS f32x4* d = (LAS f32x4*)(red + (hw * 4 + (lane >> 4)) * 64 + (lane & 3) * 8);
        d[0] = f32x4{ag[0], ag[1], ag[2], ag[3]}; d[1] = f32x4{ag[4], ag[5], ag[6], ag[7]}; d[8] = f32x4{au[0], au[1], au[2], au[3]}; d[9] = f32x4{au[4], au[5], au[6], au[7]}; }
    lds_sync(hcnt, hs0 + 4, lane);
    { float s_ = 0.f;
#pragma unroll
      for (int k = 0; k < 16; ++k) s_ += red[k * 64 + lane];
      auto sw = __builtin_amdgcn_permlane32_swap(__float_as_uint(s_), __float_as_uint(s_), false, false);
      const float g = __uint_as_float(sw[0]), uu = __uint_as_float(sw[1]);
      if (lane < 36) hv_[lane] = lane < 32 ? (unsigned)f2bf(g * __builtin_amdgcn_rcpf(1.f + __expf(-g)) * uu) : 0u; }
    asm volatile("s_waitcnt lgkmcnt(0)" ::: "memory");
    const int l = lane < 54 ? lane : 0, rg = (l * 57) >> 10;
    float y[8] = {};
#pragma unroll
    for (int i = 0; i < 11; ++i) { const unsigned hv = hv_[rg + 3 * i], hh = hv << 16;
#pragma unroll
        for (int k = 0; k < 4; ++k) { y[2 * k] = dot2bf(w.d[i][k], hv, y[2 * k]); y[2 * k + 1] = dot2bf(w.d[i][k], hh, y[2 * k + 1]); } }
#pragma unroll
    for (int k = 0; k < 8; ++k) y[k] += __shfl(y[k], (lane + 18) & 63) + __shfl(y[k], (lane + 36) & 63);
    const unsigned so = lane < 18 ? (unsigned)WS_SLAB + ((use * 144 + c) * 576 + hw * 144 + lane * 8) * 4 : OOB;
    __builtin_amdgcn_raw_buffer_store_b128(__builtin_bit_cast(u32x4, f32x4{y[0], y[1], y[2], y[3]}), rws, so, 0, 16);
    __builtin_amdgcn_raw_buffer_store_b128(__builtin_bit_cast(u32x4, f32x4{y[4], y[5], y[6], y[7]}), rws, so + 16, 0, 16);
    asm volatile("s_waitcnt vmcnt(0)" ::: "memory");
    lds_sync(hcnt, hs0 + 8, lane);
    if (t == 0) __hip_atomic_store((GAS unsigned*)(ws + WS_FF + (use * 144 + c) * 128), 1u, RLX_AGENT);
}
DI f32x4 ffn_reduce(char* ws, rsrc_t rws, int use, int rho, int hw, int lane, bool nap) {
    { unsigned sp = 0; for (;;) { bool ok = true;
#pragma unroll
        for (int i = 0; i < 3; ++i) { const int c = i * 64 + lane; ok &= __hip_atomic_load((GAS unsigned*)(ws + WS_FF + (use * 144 + (c < 144 ? c : 0)) * 128), RLX_AGENT) != 0; }
        if (__all(ok) || ++sp > (1u << 20)) break; if (nap) __builtin_amdgcn_s_sleep(2); } }
    f32x4 a = {0, 0, 0, 0}; u32x4 v[5];
#pragma unroll
    for (int i = 0; i < 5; ++i) { const int c = (lane >> 1) + 32 * i; v[i] = __builtin_amdgcn_raw_buffer_load_b128(rws, c < 144 ? (unsigned)WS_SLAB + ((use * 144 + c) * 576 + rho * 32 + hw * 8 + (lane & 1) * 4) * 4 : OOB, 0, 16); }
#pragma unroll
    for (int i = 0; i < 5; ++i) a += __builtin_bit_cast(f32x4, v[i]);
#pragma unroll
    for (int k = 0; k < 4; ++k) a[k] = xg_sum(dpp_add<0x128>(dpp_add<0x124>(dpp_add<0x122>(a[k]))));
    return a;
}
DI float sigmoidf_(float x) { return __builtin_amdgcn_rcpf(1.f + __expf(-x)); }
DI void patch_key(char* lds, int sel, int lane) {
    LAS const float* kn = (LAS const float*)(lds + SP_KVN); LAS char* t = (LAS char*)lds + (sel >> 5) * TILE_B; const int rr = sel & 31;
    if (lane < 16) *(LAS u16*)(t + ((lane >> 3) * 32 + rr) * 16 + (lane & 7) * 2) = f2bf(kn[lane]);
    else if (lane < 18) *(LAS u16*)(t + K2_OFF + rr * 4 + (lane - 16) * 2) = f2bf(kn[lane]);
    else if (lane < 36) *(LAS u16*)(t + V_OFF + (rr >> 3) * VSUB + (lane - 18) * 16 + (rr & 7) * 2) = f2bf(kn[lane]);
}

__global__ __launch_bounds__(768) void k_attn1(Params p) {
    extern __shared__ __attribute__((aligned(16))) char lds[];
    const AttnCtx c = attn_ctx(lds);
    const int tid = c.tid, lane = c.lane, wave = c.wave, kh = c.kh, m = c.m, g = c.g, rt = c.rt, r16 = c.r16, gq = c.gq, T2 = c.T2, r = c.r, h = c.h;
    STAMP_DECL; STAMP(2, 0);
    LAS unsigned* W = (LAS unsigned*)(lds + SP_W);
    if (tid < 16) W[tid] = 0;
    BAR_LDS();
    const char* kv = p.ws + WS_KV + KVP_B;
    const rsrc_t rws = mkrs(p.ws, WS_END);
    GAS unsigned* flags = (GAS unsigned*)(p.ws + WS_FLAG) + (size_t)g * 8 * 64;
    const unsigned xo = (unsigned)WS_XO + g * XO_G;
    if (wave < 8) {
        FragQ q; load_q_stage1(c, p.ws + WS_QF + QF_B + ((size_t)c.b * 8 + c.pp * 2) * 1152, kv, q);
        lds_sync(W + W_ASYNC, 8u, lane);
        STAMP(2, 1);
        f32x16 oa = {}, ob = {};
        attn_pass((LAS const char*)lds, kv, c.lds0, wave, lane, q, oa, ob, W + W_ASYNC, []() {});
        STAMP(2, 2);
        { unsigned sp = 0; while (__builtin_amdgcn_readfirstlane(__hip_atomic_load(W + W_READY, __ATOMIC_RELAXED, __HIP_MEMORY_SCOPE_WORKGROUP)) == 0 && ++sp < (1u << 24)) __builtin_amdgcn_s_sleep(1); }
        asm volatile("" ::: "memory");
        const int sel = __builtin_amdgcn_readfirstlane(W[W_SEL]); const bool owner = (sel >> 6) == g;
        bool bad;
        { LAS const char* sp = (LAS const char*)lds + SP_T + (kh == ((sel >> 5) & 1) ? 0 : 512);
          const bf16x8 k1 = *(LAS const bf16x8*)(sp + (r == 0 ? h * 16 : 48)); u32x4 k2u = {*(LAS const unsigned*)(sp + (r == 0 ? 32 : 48)), 0, 0, 0};
          const bf16x8 v0 = *(LAS const bf16x8*)(sp + (h == 0 ? 64 + (r < 19 ? r : 19) * 16 : 48)); const f32x16 zero = {};
          f32x16 sa = MFMA32(k1, q.a1, zero); sa = MFMA32(__builtin_bit_cast(bf16x8, k2u), q.a2, sa);
          f32x16 sb = MFMA32(k1, q.b1, zero); sb = MFMA32(__builtin_bit_cast(bf16x8, k2u), q.b2, sb);
#pragma unroll
          for (int j = 0; j < 8; ++j) { sa[j] = __builtin_amdgcn_exp2f(sa[j]); sb[j] = __builtin_amdgcn_exp2f(sb[j]); }
          oa = pv(v0, lo8(sa), oa); ob = pv(v0, lo8(sb), ob);
          bad = o_bad(oa, h) || o_bad(ob, h) || (h == 0 && (!(oa[10] * 1024.f >= sa[0]) || !(ob[10] * 1024.f >= sb[0]))); }
        if (owner) { unsigned sp = 0;
            while (__builtin_amdgcn_readfirstlane(__hip_atomic_load(W + W_OZDONE, __ATOMIC_RELAXED, __HIP_MEMORY_SCOPE_WORKGROUP)) == 0 && ++sp < (1u << 24)) __builtin_amdgcn_s_sleep(1);
            asm volatile("" ::: "memory"); }
        { const bool anyb = __any(bad); if (lane == 0) W[W_BAD + wave] = anyb; put_partial(lds, c, oa, ob); }
        BAR_LDS();
        bool use_oz = owner;
        if (__builtin_expect(any_bad(lds) || W[W_OZBAD] != 0, 0)) {
            fallback_restore(c, kv, true);
            if (wave == 0) patch_key(lds, sel, lane);
            if (owner && r == (sel & 31)) { LAS const float* qn = (LAS const float*)(lds + SP_QN) + c.pp * 18; u32x4 a, z = {0, 0, 0, 0};
                for (int j = 0; j < 4; ++j) a[j] = pk2(qn[8 * h + 2 * j], qn[8 * h + 2 * j + 1]);
                z[0] = h == 0 ? pk2(qn[16], qn[17]) : 0u;
                if (((sel >> 5) & 1) == 0) { q.a1 = __builtin_bit_cast(bf16x8, a); q.a2 = __builtin_bit_cast(bf16x8, z); } else { q.b1 = __builtin_bit_cast(bf16x8, a); q.b2 = __builtin_bit_cast(bf16x8, z); } }
            BAR_LDS();
            fallback_pass(lds, c, q, oa, ob);
            use_oz = false;
        }
        STAMP(2, 3);
        { f32x4 a, e; f32x2 z; finish_o(lds, c, oa, ob, a, e, z);
          if (use_oz && kh == ((sel >> 5) & 1) && r == (sel & 31)) { LAS const float* oz = (LAS const float*)(lds + SP_OZ) + c.pp * 20;
              a = *(LAS const f32x4*)(oz + 4 * h); e = *(LAS const f32x4*)(oz + 8 + 4 * h); z = f32x2{oz[16], oz[17]}; }
          publish_o(rws, c, a, e, z); }
        STAMP(2, 4);
        BAR_ALL();
        if (tid == 0) __hip_atomic_store(flags + m * 64, 2u, RLX_AGENT);
        const unsigned aoff = ((4 * 9 * kh + gq) * 64 + rt * 16 + r16) * 16;
        unsigned coff[3]; f32x4 res[3]; u32x2 wrp[3];
#pragma unroll
        for (int i = 0; i < 3; ++i) { const int n0 = 16 * (kh == 0 ? i : 3 + i) + 4 * gq, n = n0 < 72 ? n0 : 68;
            res[i] = *(const f32x4*)((const float*)(p.ws + WS_X2) + (size_t)T2 * D + 72 * m + n);
            if (T2 == sel) res[i] = *(LAS const f32x4*)((LAS const float*)(lds + SP_XS) + 72 * m + n);
            wrp[i] = *(const u32x2*)(p.wr + 72 * m + n);
            coff[i] = n0 < 72 ? T2 * D + 72 * m + n0 : OOB / 2; PIN(coff[i]); }
        asm volatile("s_barrier" ::: "memory");
        STAMP(2, 5);
        bf16x8 af[9];
#pragma unroll
        for (int s = 0; s < 9; ++s) af[s] = __builtin_bit_cast(bf16x8, __builtin_amdgcn_raw_buffer_load_b128(rws, aoff, xo + s * 4096, 16));
        BAR_ALL();
        STAMP(2, 6);
        LAS const char* bb = (LAS const char*)lds + B1_OFF + kh * 45 * 1024 + lane * 16;
        f32x4 x3[3];
        { f32x4 acc[5] = {}; gemm45(acc, af, [&](int s, int ct) { return *(LAS const bf16x8*)(bb + (s * 5 + ct) * 1024); }); STAMP(2, 7);
          khalf_reduce((LAS f32x4*)lds, acc, x3, rt, kh, lane); }
        float sx = 0.f, sq = 0.f, sw = 0.f;
#pragma unroll
        for (int i = 0; i < 3; ++i) { x3[i] += res[i]; const bool ok = coff[i] < OOB / 2;
            const f32x4 x = ok ? x3[i] : f32x4{0, 0, 0, 0};
            sx += (x[0] + x[1]) + (x[2] + x[3]); sq += x[0] * x[0] + x[1] * x[1] + x[2] * x[2] + x[3] * x[3];
            sw += x[0] * bflo(wrp[i][0]) + x[1] * bfhi(wrp[i][0]) + x[2] * bflo(wrp[i][1]) + x[3] * bfhi(wrp[i][1]); }
        sx = xg_sum(sx); sq = xg_sum(sq); sw = xg_sum(sw);
        LAS float* st = (LAS float*)(lds + 24576);
        if (gq == 0) *(LAS f32x4*)(st + (kh * 64 + rt * 16 + r16) * 4) = f32x4{sx, sq, sw, 0.f};
        BAR_LDS();
        if (tid < 192) { const int row = (tid * 171) >> 9, cp = tid - row * 3;
            put_granule(p.ws, (unsigned)WS_GST + ((g * 8 + m) * 192 + tid) * 8, __float_as_uint(st[row * 4 + cp] + st[(64 + row) * 4 + cp]));
            unsigned v[8]; get_granules<8>(p.ws, (unsigned)WS_GST + (g * 8 * 192 + tid) * 8, 192 * 8, true, v, false);
            float t_ = 0.f;
#pragma unroll
            for (int k = 0; k < 8; ++k) t_ += __uint_as_float(v[k]);
            st[512 + row * 4 + cp] = t_; }
        BAR_LDS();
        STAMP(2, 8);
        const float wsum = __uint_as_float(W[W_WSUM]);
        int best; float bestr;
        { const f32x4 t = *(LAS const f32x4*)(st + 512 + lane * 4); const float mu_ = t[0] * (1.f / D), rs_ = rsqrtf(fmaxf(t[1] * (1.f / D) - mu_ * mu_, 0.f) + 1e-5f);
          best = wave_argmax(rs_ * (t[2] - mu_ * wsum), bestr); }
        if (m == 0 && wave == 0 && lane < 8)
            __hip_atomic_store((GAS unsigned long long*)(p.ws + WS_GCAND + (lane * 32 + g) * 8), ((unsigned long long)(0x100u | best) << 32) | __float_as_uint(bestr), RLX_AGENT);
        const f32x4 t = *(LAS const f32x4*)(st + 512 + (rt * 16 + r16) * 4);
        const float mu = t[0] * (1.f / D), rs = rsqrtf(fmaxf(t[1] * (1.f / D) - mu * mu, 0.f) + 1e-5f);
        const bool cand = rt * 16 + r16 == best;
        const rsrc_t rout = mkrs(p.out, S * D * 2);
        f32x4 xn[3], x4[3];
#pragma unroll
        for (int i = 0; i < 3; ++i) { xn[i] = (x3[i] - mu) * rs; x4[i] = x3[i] + xn[i]; }
        if (rt == (best >> 4)) {
#pragma unroll
            for (int i = 0; i < 3; ++i) { const int n = 16 * (kh == 0 ? i : 3 + i) + 4 * gq;
                if (cand && n < 72) { const unsigned gx = (unsigned)WS_GCXN + ((g * 8 + m) * 36 + n / 2) * 8, g4 = (unsigned)WS_GCX4 + ((g * 8 + m) * 72 + n) * 8;
                    put_granule(p.ws, gx, pk2(xn[i][0], xn[i][1])); put_granule(p.ws, gx + 8, pk2(xn[i][2], xn[i][3]));
                    for (int j = 0; j < 4; ++j) put_granule(p.ws, g4 + j * 8, __float_as_uint(x4[i][j])); } } }
#pragma unroll
        for (int i = 0; i < 3; ++i) { u32x2 w = {pk2(x4[i][0], x4[i][1]), pk2(x4[i][2], x4[i][3])};
            __builtin_amdgcn_raw_buffer_store_b64(w, rout, (coff[i] < OOB / 2 && !cand) ? coff[i] * 2 : OOB, 0, 0); }
        STAMP(2, 9);
        STAMP_FLUSH(2);
        return;
    }
    __builtin_amdgcn_s_setprio(3);
    const int t = tid - 512, hw = wave - 8;
    LAS unsigned* hcnt = W + W_HSYNC; unsigned hs = 0;
    const bool isF = (c.b >> 3) < 18, isR = !isF && ((c.b >> 3) - 18) * 8 + (c.b & 7) < 18;
    const int fc = (c.b & 7) * 18 + (c.b >> 3), rho = ((c.b >> 3) - 18) * 8 + (c.b & 7);
    int sel; float rsel;
    { const u32x2 cd = *(const u32x2*)(p.ws + WS_CAND0 + (lane & 31) * 8); const int i = wave_argmax(__uint_as_float(cd[0]), rsel); sel = __builtin_amdgcn_readlane((int)cd[1], i); }
    const bool owner = (sel >> 6) == g;
    unsigned xv[9];
    if (isF) {
#pragma unroll
        for (int i = 0; i < 9; ++i) xv[i] = __builtin_amdgcn_raw_buffer_load_b16(rws, (t >> 2) * 2 + i * 128, (unsigned)WS_XN + sel * (D * 2), 0); }
    if (hw == 3) { float s_ = 0.f; for (int i = 0; i < 9; ++i) s_ += bf2f(p.wr[i * 64 + lane]); s_ = wave_sum(s_); if (lane == 0) W[W_WSUM] = __float_as_uint(s_); }
    while (__builtin_amdgcn_readfirstlane(__hip_atomic_load(W + W_ASYNC, __ATOMIC_RELAXED, __HIP_MEMORY_SCOPE_WORKGROUP)) < 8u) __builtin_amdgcn_s_sleep(2);
    STAMP(2, 1);
    if (isF) { FfnW w0; ffn_load(w0, p, fc, t, hw, lane); STAMP(2, 2); ffn_partial(w0, xv, lds, p.ws, rws, 0, fc, t, hw, lane, hs); hs += 8; }
    STAMP(2, 3);
    if (isR) { const f32x4 xo_ = *(const f32x4*)((const float*)(p.ws + WS_X2) + (size_t)sel * D + rho * 32 + hw * 8 + (lane & 1) * 4);
        const f32x4 y = ffn_reduce(p.ws, rws, 0, rho, hw, lane, true); const float sg = sigmoidf_(rsel);
        if (lane < 8) put_granule(p.ws, (unsigned)WS_GROW + (rho * 32 + hw * 8 + (lane & 1) * 4 + (lane >> 1)) * 8, __float_as_uint(xo_[lane >> 1] + y[lane >> 1] * sg)); }
    STAMP(2, 4);
    if (owner) {
        u32x4 wf[5][5];
#pragma unroll
        for (int i = 0; i < 5; ++i)
#pragma unroll
            for (int ct = 0; ct < 5; ++ct) { const int Sx = hw + 4 * i < 18 ? hw + 4 * i : hw; wf[i][ct] = *(const u32x4*)(p.ws + WS_WQF + ((size_t)(m * 90 + Sx * 5 + ct) * 64 + lane) * 16); }
        LAS float* xrow = (LAS float*)(lds + SP_XS);
        unsigned v[9]; get_granules<9>(p.ws, (unsigned)WS_GROW + lane * 8, 64 * 8, true, v, true);
        float s1 = 0.f, s2 = 0.f;
#pragma unroll
        for (int i = 0; i < 9; ++i) { const float x = __uint_as_float(v[i]); xrow[i * 64 + lane] = x; s1 += x; s2 += x * x; }
        s1 = wave_sum(s1); s2 = wave_sum(s2);
        const float mu = s1 * (1.f / D), rs = rsqrtf(fmaxf(s2 * (1.f / D) - mu * mu, 0.f) + 1e-5f);
        f32x4 acc[5] = {};
#pragma unroll
        for (int i = 0; i < 5; ++i) { const int Sx = hw + 4 * i; if (Sx < 18) { const f32x4 a = *(LAS const f32x4*)(xrow + 32 * Sx + 8 * gq), e = *(LAS const f32x4*)(xrow + 32 * Sx + 8 * gq + 4);
            const u32x4 xb = {pk2((a[0] - mu) * rs, (a[1] - mu) * rs), pk2((a[2] - mu) * rs, (a[3] - mu) * rs), pk2((e[0] - mu) * rs, (e[1] - mu) * rs), pk2((e[2] - mu) * rs, (e[3] - mu) * rs)};
#pragma unroll
            for (int ct = 0; ct < 5; ++ct) acc[ct] = MFMA16(__builtin_bit_cast(bf16x8, wf[i][ct]), __builtin_bit_cast(bf16x8, xb), acc[ct]); } }
        LAS float* scr = (LAS float*)(lds + SP_SCR);
        if (r16 == 0) {
#pragma unroll
            for (int ct = 0; ct < 5; ++ct) *(LAS f32x4*)(scr + hw * 80 + ct * 16 + gq * 4) = acc[ct]; }
        lds_sync(hcnt, hs += 4, lane);
        if (hw == 1) { const int n = lane + 8;
            if (lane < 8) { const int tt = 72 + lane; const float v_ = scr[tt] + scr[80 + tt] + scr[160 + tt] + scr[240 + tt];
                const int idx = m < 4 ? (m >> 1) * 18 + (m & 1) * 8 + lane : ((m == 4 || m == 5) && lane >= 6) ? (m - 4) * 18 + 10 + lane : -1;
                if (idx >= 0) for (int rep = 0; rep < 16; ++rep) put_granule(p.ws, (unsigned)WS_GKV + (rep * 64 + idx) * 8, __float_as_uint(v_)); } (void)n; }
        if (t < 72) ((LAS float*)(lds + SP_QN))[t] = (scr[t] + scr[80 + t] + scr[160 + t] + scr[240 + t]) * QSCALE;
    }
    STAMP(2, 5);
    if (hw == 0) {
        unsigned v[1]; get_granules<1>(p.ws, (unsigned)WS_GKV + ((c.b & 15) * 64 + lane) * 8, 0, lane < 36, v, true);
        LAS float* kn = (LAS float*)(lds + SP_KVN); if (lane < 36) kn[lane] = __uint_as_float(v[0]);
        while (__builtin_amdgcn_readfirstlane(__hip_atomic_load(W + W_ASYNC, __ATOMIC_RELAXED, __HIP_MEMORY_SCOPE_WORKGROUP)) < 16u) __builtin_amdgcn_s_sleep(4);
        asm volatile("" ::: "memory");
        LAS char* sp = (LAS char*)lds + SP_T; *(LAS u32x4*)(sp + lane * 16) = u32x4{0, 0, 0, 0};
        LAS const char* kt = (LAS const char*)lds + (sel >> 5) * TILE_B; const int rr_ = sel & 31;
        asm volatile("s_waitcnt lgkmcnt(0)" ::: "memory");
        if (lane < 16) { *(LAS u16*)(sp + lane * 2) = *(LAS const u16*)(kt + ((lane >> 3) * 32 + rr_) * 16 + (lane & 7) * 2); *(LAS u16*)(sp + 512 + lane * 2) = f2bf(kn[lane]); }
        else if (lane < 18) { *(LAS u16*)(sp + 32 + (lane - 16) * 2) = *(LAS const u16*)(kt + K2_OFF + rr_ * 4 + (lane - 16) * 2); *(LAS u16*)(sp + 512 + 32 + (lane - 16) * 2) = f2bf(kn[lane]); }
        else if (lane < 36) { const int d = lane - 18; *(LAS u16*)(sp + 64 + d * 16) = (u16)(*(LAS const u16*)(kt + V_OFF + (rr_ >> 3) * VSUB + d * 16 + (rr_ & 7) * 2) ^ 0x8000u); *(LAS u16*)(sp + 512 + 64 + d * 16) = f2bf(kn[lane]); }
        else if (lane == 36) { *(LAS u16*)(sp + 64 + 18 * 16) = 0xBF80u; *(LAS u16*)(sp + 512 + 64 + 18 * 16) = 0x3F80u; }
        if (lane == 0) W[W_SEL] = sel;
        asm volatile("s_waitcnt lgkmcnt(0)" ::: "memory");
        if (lane == 0) __hip_atomic_store(W + W_READY, 1u, __ATOMIC_RELAXED, __HIP_MEMORY_SCOPE_WORKGROUP);
    }
    STAMP(2, 6);
    if (owner) {
        lds_sync(hcnt, hs += 4, lane);
        LAS const float* qn = (LAS const float*)(lds + SP_QN); const int hd = r < 2 ? r : (r == 4 || r == 5) ? r - 2 : -1;
        u32x4 a = {0, 0, 0, 0}, z = {0, 0, 0, 0};
        if (hd >= 0) { for (int j = 0; j < 4; ++j) a[j] = pk2(qn[hd * 18 + 8 * h + 2 * j], qn[hd * 18 + 8 * h + 2 * j + 1]); if (h == 0) z[0] = pk2(qn[hd * 18 + 16], qn[hd * 18 + 17]); }
        const bf16x8 qz1 = __builtin_bit_cast(bf16x8, a), qz2 = __builtin_bit_cast(bf16x8, z);
        LAS char* pz = (LAS char*)lds + SP_PZ + hw * 256;
        LAS const char* zc = (LAS const char*)lds + SP_T + 48;
        LAS const char* pr0 = r < 4 ? pz + r * 64 + h * 16 : zc; const int pstep = r < 4 ? 32 : 0;
        const f32x16 zero = {}; f32x16 oz = {}; const int dsel = r < 19 ? r : 19;
        LAS char* pw = pz + 2 * h * 64 + r * 2;
        LAS const char* tk = (LAS const char*)lds + hw * 16 * TILE_B + lane * 16;
        LAS const char* tk2 = (LAS const char*)lds + hw * 16 * TILE_B + K2_OFF + r * 4;
        LAS const char* tv = (LAS const char*)lds + hw * 16 * TILE_B + V_OFF + h * VSUB + dsel * 16;
        const int kill = (r == (sel & 31)) ? (sel >> 5) - hw * 16 : -1;
        f32x16 sc_; { u32x4 k2u = {*(LAS const unsigned*)tk2, 0, 0, 0}; sc_ = MFMA32(qz1, *(LAS const bf16x8*)tk, zero); sc_ = MFMA32(qz2, __builtin_bit_cast(bf16x8, k2u), sc_); }
        for (int i = 0; i < 16; ++i) {
            float p0 = __builtin_amdgcn_exp2f(sc_[0]), p1 = __builtin_amdgcn_exp2f(sc_[1]);
            if (i == kill) { p0 = 0.f; p1 = 0.f; }
            const unsigned pp_ = pk2(p0, p1);
            asm volatile("" ::: "memory");
            *(LAS u16*)pw = (u16)pp_; *(LAS u16*)(pw + 64) = (u16)(pp_ >> 16);
            asm volatile("" ::: "memory");
            const bf16x8 v0 = *(LAS const bf16x8*)tv, v1 = *(LAS const bf16x8*)(tv + 2 * VSUB);
            tk += TILE_B; tk2 += TILE_B; tv += TILE_B;
            { u32x4 k2u = {*(LAS const unsigned*)tk2, 0, 0, 0}; const bf16x8 k1 = *(LAS const bf16x8*)tk;
              const bf16x8 b0 = *(LAS const bf16x8*)pr0, b1 = *(LAS const bf16x8*)(pr0 + pstep);
              sc_ = MFMA32(qz1, k1, zero); sc_ = MFMA32(qz2, __builtin_bit_cast(bf16x8, k2u), sc_);
              oz = MFMA32(v0, b0, oz); oz = MFMA32(v1, b1, oz); } }
        if (hw == 0) {
            LAS const char* sp = (LAS const char*)lds + SP_T + 512;
            u32x4 k2u = {*(LAS const unsigned*)(sp + (r == 0 ? 32 : 48)), 0, 0, 0};
            sc_ = MFMA32(qz1, *(LAS const bf16x8*)(sp + (r == 0 ? h * 16 : 48)), zero); sc_ = MFMA32(qz2, __builtin_bit_cast(bf16x8, k2u), sc_);
            const unsigned pp_ = pk2(__builtin_amdgcn_exp2f(sc_[0]), __builtin_amdgcn_exp2f(sc_[1]));
            asm volatile("" ::: "memory");
            *(LAS u16*)pw = (u16)pp_; *(LAS u16*)(pw + 64) = (u16)(pp_ >> 16);
            asm volatile("" ::: "memory");
            oz = MFMA32(*(LAS const bf16x8*)(sp + (h == 0 ? 64 + dsel * 16 : 48)), *(LAS const bf16x8*)pr0, oz); }
        LAS float* scr = (LAS float*)(lds + SP_SCR);
        if (r < 4) {
#pragma unroll
            for (int j = 0; j < 11; ++j) scr[(hw * 11 + j) * 8 + h * 4 + r] = oz[j]; }
        lds_sync(hcnt, hs += 4, lane);
        if (hw == 0) { LAS float* ozf = (LAS float*)(lds + SP_OZ); const int hd2 = lane & 3; bool bad = false;
            const float lsum = scr[(0 * 11 + 10) * 8 + hd2] + scr[(1 * 11 + 10) * 8 + hd2] + scr[(2 * 11 + 10) * 8 + hd2] + scr[(3 * 11 + 10) * 8 + hd2];
            bad |= !(lsum >= 0x1p-60f && lsum <= 0x1p59f); const float il = 1.f / lsum;
#pragma unroll
            for (int rd = 0; rd < 2; ++rd) { const int d = rd * 16 + (lane >> 2); if (d < 18) { const int reg = (d & 3) + 4 * (d >> 3), hh = (d >> 2) & 1, o_ = reg * 8 + hh * 4 + hd2;
                const float s_ = scr[o_] + scr[88 + o_] + scr[176 + o_] + scr[264 + o_]; bad |= !(fabsf(s_) < INFINITY); ozf[hd2 * 20 + d] = s_ * il; } }
            if (__any(bad) && lane == 0) W[W_OZBAD] = 1;
            asm volatile("s_waitcnt lgkmcnt(0)" ::: "memory");
            if (lane == 0) __hip_atomic_store(W + W_OZDONE, 1u, __ATOMIC_RELAXED, __HIP_MEMORY_SCOPE_WORKGROUP); }
    }
    FfnW w = {};
    if (isF) ffn_load(w, p, fc, t, hw, lane);
    STAMP(2, 10);
    BAR_LDS();
    if (__builtin_expect(any_bad(lds) || W[W_OZBAD] != 0, 0)) { fallback_restore(c, kv, false); BAR_LDS(); BAR_LDS(); BAR_LDS(); BAR_LDS(); }
    asm volatile("s_waitcnt lgkmcnt(0)\n\ts_barrier" ::: "memory");
    if (hw != 0) stage_frags(c, p.ws + WS_WOF + (size_t)m * FRAGS_B, hw - 1, 3); else wait_flags8(flags, 2u, lane);
    asm volatile("s_barrier" ::: "memory");
    BAR_ALL();
    BAR_LDS(); BAR_LDS(); BAR_LDS();
    int sel2, crow; float rsel2;
    { unsigned long long x; unsigned sp = 0;
      for (;;) { x = ld_granule(p.ws, (unsigned)WS_GCAND + ((c.b & 7) * 32 + (lane & 31)) * 8); if (__all((x >> 32) != 0) || ++sp > (1u << 20)) break; }
      const int gi = wave_argmax(__uint_as_float((unsigned)x), rsel2) & 31; const int row = (int)((x >> 32) & 63);
      sel2 = gi * 64 + __builtin_amdgcn_readlane(row, gi); crow = g * 64 + __builtin_amdgcn_readlane(row, g); }
    const int g2 = sel2 >> 6;
    STAMP(2, 11);
    if (isF) {
        unsigned v[9], xw[9]; unsigned sp = 0;
        for (;;) { bool ok = true;
#pragma unroll
            for (int i = 0; i < 9; ++i) { const int col = (t >> 2) + 64 * i; const unsigned long long x = ld_granule(p.ws, (unsigned)WS_GCXN + (g2 * 288 + (col >> 1)) * 8); v[i] = (unsigned)x; ok &= (x >> 32) != 0; }
            if (__all(ok) || ++sp > (1u << 20)) break; }
#pragma unroll
        for (int i = 0; i < 9; ++i) xw[i] = ((t >> 2) & 1) ? v[i] >> 16 : v[i] & 0xFFFFu;
        ffn_partial(w, xw, lds, p.ws, rws, 1, fc, t, hw, lane, hs); hs += 8;
    }
    if (hw == 3 && crow != sel2) {
        unsigned v[2]; get_granules<2>(p.ws, (unsigned)WS_GCX4 + ((g * 8 + m) * 72 + 2 * lane) * 8, 8, lane < 36, v, false);
        if (lane < 36) *(unsigned*)(p.out + (size_t)crow * D + 72 * m + 2 * lane) = pk2(__uint_as_float(v[0]), __uint_as_float(v[1])); }
    STAMP(2, 12);
    if (isR) { unsigned v[1]; const int col = rho * 32 + hw * 8 + (lane & 1) * 4 + ((lane >> 1) & 3), mm = col / 72;
        get_granules<1>(p.ws, (unsigned)WS_GCX4 + ((g2 * 8 + mm) * 72 + col - mm * 72) * 8, 0, lane < 8, v, false);
        const f32x4 y = ffn_reduce(p.ws, rws, 1, rho, hw, lane, false);
        if (lane < 8) p.out[(size_t)sel2 * D + col] = f2bf(__uint_as_float(v[0]) + y[lane >> 1] * sigmoidf_(rsel2)); }
    STAMP(2, 13);
    STAMP_FLUSH(2);
}

extern "C" void kernel_launch(void* const* d_in, const int* in_sizes, int n_in, void* d_out, int out_size, void* d_ws, size_t ws_size, hipStream_t stream) {
    Params p; p.x = (const u16*)d_in[0]; p.wq = (const u16*)d_in[1]; p.wk = (const u16*)d_in[2]; p.wv = (const u16*)d_in[3]; p.wo = (const u16*)d_in[4];
    p.wr = (const u16*)d_in[6]; p.wg = (const u16*)d_in[7]; p.wu = (const u16*)d_in[8]; p.wd = (const u16*)d_in[9]; p.out = (u16*)d_out; p.ws = (char*)d_ws;
    (void)hipFuncSetAttribute((const void*)k_attn0, hipFuncAttributeMaxDynamicSharedMemorySize, ATT_LDS);
    (void)hipFuncSetAttribute((const void*)k_attn1, hipFuncAttributeMaxDynamicSharedMemorySize, ATT_LDS);
    (void)hipFuncSetAttribute((const void*)k_qkv, hipFuncAttributeMaxDynamicSharedMemorySize, QKV_LDS);
    k_qkv<<<256, 512, QKV_LDS, stream>>>(p);
    k_attn0<<<256, 512, ATT_LDS, stream>>>(p);
    k_attn1<<<256, 768, ATT_LDS, stream>>>(p);
}
```

```cpp
#include <hip/hip_runtime.h>
#include <hip/hip_bf16.h>
#include <stdint.h>

typedef __bf16 bf16x8 __attribute__((ext_vector_type(8)));
typedef __bf16 bf16x16 __attribute__((ext_vector_type(16)));
typedef short s16x4 __attribute__((ext_vector_type(4)));
typedef float f32x16 __attribute__((ext_vector_type(16)));
typedef float f32x8 __attribute__((ext_vector_type(8)));
typedef float f32x4 __attribute__((ext_vector_type(4)));
typedef unsigned u32x4 __attribute__((ext_vector_type(4)));
typedef unsigned u32x2 __attribute__((ext_vector_type(2)));
typedef unsigned short u16;
#define LAS __attribute__((address_space(3)))
#define GAS __attribute__((address_space(1)))
#define DI __device__ __forceinline__
#define RLX_AGENT __ATOMIC_RELAXED, __HIP_MEMORY_SCOPE_AGENT

constexpr int S = 2048, D = 576, F = 4608;
constexpr int VSUB = 320;
constexpr int TILE_B = 1024 + 128 + 4 * VSUB;
constexpr int KV_B = 64 * TILE_B;
constexpr int K2_OFF = 1024, V_OFF = 1152;
constexpr float QSCALE = 0.2357022603955158f * 1.4426950408889634f;
constexpr int FRAGS_B = 90 * 1024;
constexpr int XO_G = 72 * 64 * 16;

constexpr size_t QF_B = (size_t)2048 * 1152, KVP_B = KV_B + 8192;
constexpr size_t WS_FLAG = 0;
constexpr size_t WS_FF = 65536;
constexpr size_t WS_GROW = WS_FF + 2 * 144 * 128;
constexpr size_t WS_GKV = WS_GROW + 576 * 8;
constexpr size_t WS_GCAND = WS_GKV + 16 * 64 * 8;
constexpr size_t WS_GST = WS_GCAND + 8 * 32 * 8;
constexpr size_t WS_GCXN = WS_GST + 32 * 8 * 192 * 8;
constexpr size_t WS_GCX4 = WS_GCXN + 32 * 8 * 36 * 8;
constexpr size_t WS_ZEND = WS_GCX4 + 32 * 8 * 72 * 8;
constexpr size_t ZERO_B = 1u << 20;
static_assert(WS_ZEND <= ZERO_B, "zeroed region");
constexpr size_t WS_QF = ZERO_B;
constexpr size_t WS_KV = WS_QF + 2 * QF_B;
constexpr size_t WS_WOF = WS_KV + 2 * KVP_B;
constexpr size_t WS_WQF = WS_WOF + 8 * FRAGS_B;
constexpr unsigned PENDING = 0xFFFFFFFFu;
constexpr size_t WS_XO = WS_WQF + 8 * FRAGS_B;
constexpr size_t WS_XX = WS_XO + 32 * XO_G;
constexpr size_t WS_SLAB = WS_XX + 32 * XO_G;
constexpr size_t WS_PEND_END = WS_SLAB + 2 * 144 * 576 * 4;
constexpr size_t WS_XO1 = WS_PEND_END;
constexpr size_t WS_ST = WS_XO1 + 32 * XO_G;
constexpr size_t WS_CAND0 = WS_ST + S * 8;
constexpr size_t WS_CSP = WS_CAND0 + 256;
constexpr size_t WS_WF = WS_CSP + 8 * 90 * 16 * 4;
constexpr size_t WS_END = WS_WF + (size_t)144 * 108 * 1024;

struct Params {
    const u16 *x, *wq, *wk, *wv, *wo, *wr, *wg, *wu, *wd; u16* out; char* ws;
};

#define SBAR() __builtin_amdgcn_sched_barrier(0)
#define PIN(x) asm volatile("" : "+v"(x))
#define MFMA32(a, b, c) __builtin_amdgcn_mfma_f32_32x32x16_bf16((a), (b), (c), 0, 0, 0)
#define MFMA16(a, b, c) __builtin_amdgcn_mfma_f32_16x16x32_bf16((a), (b), (c), 0, 0, 0)
DI f32x16 pv(bf16x8 v, f32x8 p, f32x16 o) { return __builtin_amdgcn_smfmac_f32_32x32x32_bf16(v, __builtin_bit_cast(bf16x16, p), o, (int)0xDDDDDDDDu, 0, 0); }
DI f32x8 lo8(const f32x16& x) { return __builtin_shufflevector(x, x, 0, 1, 2, 3, 4, 5, 6, 7); }
DI f32x8 hi8(const f32x16& x) { return __builtin_shufflevector(x, x, 8, 9, 10, 11, 12, 13, 14, 15); }
DI float bf2f(u16 b) { return __uint_as_float((unsigned)b << 16); }
DI float bflo(unsigned u) { return __uint_as_float(u << 16); }
DI float bfhi(unsigned u) { return __uint_as_float(u & 0xFFFF0000u); }
DI u16 f2bf(float f) { return __builtin_bit_cast(u16, (__bf16)f); }
typedef __bf16 bf16x2 __attribute__((ext_vector_type(2)));
typedef float f32x2 __attribute__((ext_vector_type(2)));
DI unsigned pk2(float lo, float hi) { return __builtin_bit_cast(unsigned, __builtin_convertvector(f32x2{lo, hi}, bf16x2)); }
DI void glds16(const void* g, unsigned lds_base) {
    unsigned sv; asm volatile("s_mov_b32 %0, m0\n\ts_mov_b32 m0, %2\n\ts_nop 0\n\tglobal_load_lds_dwordx4 %1, off\n\ts_mov_b32 m0, %0" : "=&s"(sv) : "v"(g), "s"(lds_base) : "memory"); }
DI void glds16s(const char* sbase, unsigned voff, unsigned lds_base) {
    unsigned sv; asm volatile("s_mov_b32 %0, m0\n\ts_mov_b32 m0, %3\n\ts_nop 0\n\tglobal_load_lds_dwordx4 %1, %2\n\ts_mov_b32 m0, %0" : "=&s"(sv) : "v"(voff), "s"(sbase), "s"(lds_base) : "memory"); }
DI void glds4s(const char* sbase, unsigned voff, unsigned lds_base) {
    unsigned sv; asm volatile("s_mov_b32 %0, m0\n\ts_mov_b32 m0, %3\n\ts_nop 0\n\tglobal_load_lds_dword %1, %2\n\ts_mov_b32 m0, %0" : "=&s"(sv) : "v"(voff), "s"(sbase), "s"(lds_base) : "memory"); }
DI void glds4v(const void* g, unsigned lds_base) {
    unsigned sv; asm volatile("s_mov_b32 %0, m0\n\ts_mov_b32 m0, %2\n\ts_nop 0\n\tglobal_load_lds_dword %1, off\n\ts_mov_b32 m0, %0" : "=&s"(sv) : "v"(g), "s"(lds_base) : "memory"); }
typedef short v4i16_t __attribute__((ext_vector_type(4)));
DI s16x4 trread(LAS const char* p) { return __builtin_bit_cast(s16x4, __builtin_amdgcn_ds_read_tr16_b64_v4i16((LAS v4i16_t*)p)); }
DI float xg_sum(float x) {
    auto s = __builtin_amdgcn_permlane16_swap(__float_as_uint(x), __float_as_uint(x), false, false);
    x = __uint_as_float(s[0]) + __uint_as_float(s[1]);
    auto t = __builtin_amdgcn_permlane32_swap(__float_as_uint(x), __float_as_uint(x), false, false);
    return __uint_as_float(t[0]) + __uint_as_float(t[1]);
}
template <int CTRL> DI float dppf(float x) { return __builtin_bit_cast(float, __builtin_amdgcn_mov_dpp(__builtin_bit_cast(int, x), CTRL, 0xf, 0xf, true)); }
template <int CTRL> DI float dpp_add(float x) { return x + dppf<CTRL>(x); }
DI float wave_sum(float x) { x = dpp_add<0x121>(x); x = dpp_add<0x122>(x); x = dpp_add<0x124>(x); x = dpp_add<0x128>(x); return xg_sum(x); }
DI float wave_max(float x) {
    x = fmaxf(x, dppf<0x121>(x)); x = fmaxf(x, dppf<0x122>(x)); x = fmaxf(x, dppf<0x124>(x)); x = fmaxf(x, dppf<0x128>(x));
    auto s = __builtin_amdgcn_permlane16_swap(__float_as_uint(x), __float_as_uint(x), false, false); x = fmaxf(__uint_as_float(s[0]), __uint_as_float(s[1]));
    auto t = __builtin_amdgcn_permlane32_swap(__float_as_uint(x), __float_as_uint(x), false, false); return fmaxf(__uint_as_float(t[0]), __uint_as_float(t[1]));
}
DI int wave_argmax(float v, float& best) {
    best = wave_max(v); const unsigned long long m = __ballot(v == best); return m ? __builtin_ctzll(m) : 0;
}
DI float router_score(float sum, float sq, float dot, float wsum) {
    const float mu = sum * (1.f / D), var = fmaxf(sq * (1.f / D) - mu * mu, 0.f);
    const float mb = bflo(f2bf(mu)), vb = bflo(f2bf(var)), sb = bflo(f2bf(sqrtf(bflo(f2bf(vb + 1e-5f)))));
    return (dot - mb * wsum) / sb;
}
DI float lo32_bcast(float x) { auto t = __builtin_amdgcn_permlane32_swap(__float_as_uint(x), __float_as_uint(x), false, false); return __uint_as_float(t[0]); }
#define STAMP_DECL
#define CYC(i)
#define STAMP(k, i)
#define STAMP_FLUSH(k)
#define BAR_LDS() asm volatile("s_waitcnt lgkmcnt(0)\n\ts_barrier" ::: "memory")
#define BAR_ALL() asm volatile("s_waitcnt vmcnt(0) lgkmcnt(0)\n\ts_barrier" ::: "memory")

DI int blk_group(int b) { return (b & 7) + 8 * (b >> 6); }
DI int blk_member(int b) { return (b >> 3) & 7; }

template <class LD> DI void gemm45(f32x4 (&acc)[5], const bf16x8 (&xf)[9], LD ld) {
    bf16x8 cur[5], nxt[5];
#pragma unroll
    for (int ct = 0; ct < 5; ++ct) cur[ct] = ld(0, ct);
#pragma unroll
    for (int s = 0; s < 9; ++s) {
        if (s < 8) {
#pragma unroll
            for (int ct = 0; ct < 5; ++ct) nxt[ct] = ld(s + 1, ct);
        }
        SBAR();
#pragma unroll
        for (int ct = 0; ct < 5; ++ct) acc[ct] = MFMA16(cur[ct], xf[s], acc[ct]);
        SBAR();
#pragma unroll
        for (int ct = 0; ct < 5; ++ct) cur[ct] = nxt[ct];
    }
}
DI void lds_sync(LAS unsigned* cnt, unsigned target, int lane) {
    asm volatile("s_waitcnt lgkmcnt(0)" ::: "memory");
    if (lane == 0) __hip_atomic_fetch_add(cnt, 1u, __ATOMIC_RELAXED, __HIP_MEMORY_SCOPE_WORKGROUP);
    while (__builtin_amdgcn_readfirstlane(__hip_atomic_load(cnt, __ATOMIC_RELAXED, __HIP_MEMORY_SCOPE_WORKGROUP)) < target) __builtin_amdgcn_s_sleep(1);
    asm volatile("" ::: "memory");
}
template <bool COUNTER = false>
DI void khalf_reduce(LAS f32x4* ex, const f32x4 (&acc)[5], f32x4 (&out)[3], int rt, int kh, int lane, LAS unsigned* cnt = nullptr) {
    if (kh == 0) { ex[(rt * 5 + 3) * 64 + lane] = acc[3]; ex[(rt * 5 + 4) * 64 + lane] = acc[4]; }
    else { ex[(rt * 5 + 0) * 64 + lane] = acc[0]; ex[(rt * 5 + 1) * 64 + lane] = acc[1]; ex[(rt * 5 + 2) * 64 + lane] = acc[2]; }
    if (COUNTER) lds_sync(cnt, 8u, lane); else BAR_LDS();
    if (kh == 0) { out[0] = acc[0] + ex[(rt * 5 + 0) * 64 + lane]; out[1] = acc[1] + ex[(rt * 5 + 1) * 64 + lane]; out[2] = acc[2] + ex[(rt * 5 + 2) * 64 + lane]; }
    else { out[0] = acc[3] + ex[(rt * 5 + 3) * 64 + lane]; out[1] = acc[4] + ex[(rt * 5 + 4) * 64 + lane]; out[2] = f32x4{0, 0, 0, 0}; }
}
typedef __amdgpu_buffer_rsrc_t rsrc_t;
DI rsrc_t mkrs(const void* p, unsigned bytes) { return __builtin_amdgcn_make_buffer_rsrc((void*)p, 0, bytes, 0x00020000); }
constexpr unsigned OOB = 0x80000000u;
DI unsigned nopend(unsigned v) { return v == PENDING ? 0x7FC07FC0u : v; }
DI unsigned nopendf(float v) { const unsigned u = __float_as_uint(v); return u == PENDING ? 0x7FC00000u : u; }
template <int N> DI bool any_pending(const u32x4 (&x)[N]) { unsigned t = 0;
#pragma unroll
    for (int i = 0; i < N; ++i) t = max(max(t, max(x[i][0], x[i][1])), max(x[i][2], x[i][3]));
    return __any(t == PENDING); }
template <int N> DI void load_checked(u32x4 (&x)[N], rsrc_t rs, unsigned voff, unsigned soff, unsigned stride) {
    for (unsigned sp = 0;; ++sp) {
        asm volatile("" ::: "memory");
#pragma unroll
        for (int i = 0; i < N; ++i) x[i] = __builtin_amdgcn_raw_buffer_load_b128(rs, voff, soff + i * stride, 16);
        if (!any_pending(x) || sp > (1u << 16)) break; }
}
DI float dot2bf(unsigned a, unsigned b, float c) { return __builtin_amdgcn_fdot2_f32_bf16(__builtin_bit_cast(bf16x2, a), __builtin_bit_cast(bf16x2, b), c, false); }
struct QkvOff { unsigned d[3][2]; unsigned v; float scl4; };
DI QkvOff qkv_offsets(size_t qf, size_t kvp, int b, int m, int T, int kh, int gq) {
    QkvOff o; const int r = T & 31; const unsigned kt = (unsigned)kvp + (T >> 5) * TILE_B;
#pragma unroll
    for (int i = 0; i < 3; ++i)
#pragma unroll
        for (int e = 0; e < 2; ++e) { const int ct = kh == 0 ? i : 3 + i, n = 16 * ct + 4 * gq + 2 * e, p = (n * 57) >> 10, d = n - 18 * p;
            const unsigned qo = (unsigned)qf + (b * 8 + p * 2 + ((T & 63) >> 5)) * 1152 + (d < 16 ? ((d >> 3) * 32 + r) * 16 + (d & 7) * 2 : 1024 + r * 4);
            unsigned ko = OOB;
            if (m < 2) ko = kt + (m * 32 + r) * 16 + (gq - 2) * 8 + e * 4; else if (m == 4 && gq == 3 && e == 1) ko = kt + K2_OFF + r * 4;
            o.d[i][e] = ct > 4 ? OOB : (n < 72 ? qo : ko); }
    o.v = OOB;
    if (kh == 1 && gq >= 2) { const unsigned vb = kt + V_OFF + (r >> 3) * VSUB + (r & 7) * 2;
        if (m == 2 || m == 3) o.v = vb + (8 * (m - 2) + 4 * (gq - 2)) * 16; else if (m == 5 && gq == 3) o.v = vb + 14 * 16; }
    o.scl4 = (kh == 1 && gq >= 2) ? 1.f : QSCALE;
    return o;
}
#define PIN_QKV(o) do { PIN(o.d[0][0]); PIN(o.d[0][1]); PIN(o.d[1][0]); PIN(o.d[1][1]); PIN(o.d[2][0]); PIN(o.d[2][1]); PIN(o.v); PIN(o.scl4); } while (0)
DI void store_qkv3(rsrc_t rs, const QkvOff& o, size_t kvp, int m, int g, int tid, const f32x4 (&a)[3]) {
#pragma unroll
    for (int i = 0; i < 3; ++i) { const float sc = i == 1 ? o.scl4 : QSCALE;
        __builtin_amdgcn_raw_buffer_store_b32(pk2(a[i][0] * sc, a[i][1] * sc), rs, o.d[i][0], 0, 0);
        __builtin_amdgcn_raw_buffer_store_b32(pk2(a[i][2] * sc, a[i][3] * sc), rs, o.d[i][1], 0, 0); }
    if (m == 2 || m == 3 || m == 5) { const unsigned lo = pk2(a[1][0], a[1][1]), hi = pk2(a[1][2], a[1][3]);
        if (m != 5) { __builtin_amdgcn_raw_buffer_store_b16((u16)lo, rs, o.v, 0, 0); __builtin_amdgcn_raw_buffer_store_b16((u16)(lo >> 16), rs, o.v + 16, 0, 0); }
        __builtin_amdgcn_raw_buffer_store_b16((u16)hi, rs, o.v + 32, 0, 0); __builtin_amdgcn_raw_buffer_store_b16((u16)(hi >> 16), rs, o.v + 48, 0, 0); }
    if (m >= 6) { const unsigned v = m == 6 ? 0x3F803F80u : 0u; u32x4 c = {v, v, v, v};
        __builtin_amdgcn_raw_buffer_store_b128(c, rs, tid < 8 ? (unsigned)kvp + (2 * g + (tid >> 2)) * TILE_B + V_OFF + (tid & 3) * VSUB + (m == 6 ? 18 : 19) * 16 : OOB, 0, 0); }
}
DI void ln_sums(const u32x4 (&xa)[9], float& sum, float& sq) {
    float s0 = 0.f, s1 = 0.f, q0 = 0.f, q1 = 0.f;
#pragma unroll
    for (int s = 0; s < 9; ++s) { s0 = dot2bf(xa[s][0], 0x3F803F80u, s0); s1 = dot2bf(xa[s][1], 0x3F803F80u, s1); s0 = dot2bf(xa[s][2], 0x3F803F80u, s0); s1 = dot2bf(xa[s][3], 0x3F803F80u, s1);
        q0 = dot2bf(xa[s][0], xa[s][0], q0); q1 = dot2bf(xa[s][1], xa[s][1], q1); q0 = dot2bf(xa[s][2], xa[s][2], q0); q1 = dot2bf(xa[s][3], xa[s][3], q1); }
    sum = xg_sum(s0 + s1); sq = xg_sum(q0 + q1);
}
DI void ln_apply(const u32x4 (&xa)[9], float mu, float sc, bf16x8 (&xf)[9]) {
    const float nb = -mu * sc;
#pragma unroll
    for (int s = 0; s < 9; ++s) { u32x4 o;
#pragma unroll
        for (int j = 0; j < 4; ++j) o[j] = pk2(__builtin_fmaf(bflo(xa[s][j]), sc, nb), __builtin_fmaf(bfhi(xa[s][j]), sc, nb));
        xf[s] = __builtin_bit_cast(bf16x8, o); }
}

constexpr int WROW = 144, WIMG = 576 * WROW, KVIMG = 576 * 16;
constexpr int QKV_LDS = WIMG + KVIMG + 4096 + 20480;
__global__ __launch_bounds__(768) void k_qkv(Params p) {
    extern __shared__ __attribute__((aligned(16))) char lds[];
    const int tid = threadIdx.x, lane = tid & 63, wave = __builtin_amdgcn_readfirstlane(tid >> 6), rt = wave & 3, kh = wave >> 2;
    const int b = blockIdx.x, g = blk_group(b), m = blk_member(b), r16 = lane & 15, gq = lane >> 4;
    const unsigned lds0 = (unsigned)(uintptr_t)lds;
    STAMP_DECL; STAMP(0, 0);
    const rsrc_t rws = mkrs(p.ws, WS_END);
    const int prow0 = 16 * (gq >> 1) + 4 * (gq & 1) + (r16 >> 2);
    if (wave >= 8) {
        const int hw = wave - 8, t = tid - 512;
        asm volatile("s_barrier" ::: "memory");
        __builtin_amdgcn_raw_buffer_store_b128(u32x4{0, 0, 0, 0}, rws, b * 4096 + t * 16, 0, 16);
        { constexpr unsigned N16 = (unsigned)((WS_PEND_END - WS_XO) / 16);
#pragma unroll
          for (unsigned i = 0; i < (N16 + 65535) / 65536; ++i) { const unsigned e = i * 65536 + b * 256 + t;
              __builtin_amdgcn_raw_buffer_store_b128(u32x4{PENDING, PENDING, PENDING, PENDING}, rws, e < N16 ? (unsigned)WS_XO + e * 16 : OOB, 0, 16); } }
        unsigned wog[8];
        const int wf = g + 32 * (hw < 3 ? hw : 0), wfc = wf < 90 ? wf : 89, fS = wfc / 5, fct = wfc - fS * 5;
        { const int c = 4 * fS + gq, mm = c / 9, cc = c - mm * 9, n = 16 * fct + r16, nc = n < 72 ? n : 71;
#pragma unroll
          for (int j = 0; j < 8; ++j) { const int head = 4 * mm + (cc < 8 ? (cc >> 1) : (j >> 1)), d = cc < 8 ? (j < 4 ? 4 * (cc & 1) + j : 8 + 4 * (cc & 1) + j - 4) : 16 + (j & 1);
              wog[j] = p.wo[(head * 18 + d) * 576 + 72 * m + nc]; } }
        if (hw < 3 && wf < 90) {
            u32x4 o = {wog[0] | (wog[1] << 16), wog[2] | (wog[3] << 16), wog[4] | (wog[5] << 16), wog[6] | (wog[7] << 16)};
            if (16 * fct + r16 >= 72) o = u32x4{0, 0, 0, 0};
            __builtin_amdgcn_raw_buffer_store_b128(o, rws, (unsigned)WS_WOF + ((m * 90 + wf) * 64 + lane) * 16, 0, 16); }
        if (hw < 3 && wf < 90) {
            LAS const char* a0 = fct < 4 ? (LAS const char*)lds + (fS * 32 + prow0) * WROW + (r16 & 3) * 8 + fct * 32
                                         : ((r16 & 3) < 2 ? (LAS const char*)lds + (fS * 32 + prow0) * WROW + (r16 & 3) * 8 + 128 : (LAS const char*)lds + WIMG + (fS * 32 + prow0) * 16 + ((r16 & 3) - 2) * 8);
            const int stp = (fct < 4 || (r16 & 3) < 2) ? WROW : 16;
            const s16x4 lo = trread(a0), hi = trread(a0 + 8 * stp);
            const u32x4 fr = __builtin_bit_cast(u32x4, __builtin_shufflevector(lo, hi, 0, 1, 2, 3, 4, 5, 6, 7));
            __builtin_amdgcn_raw_buffer_store_b128(fr, rws, (unsigned)WS_WQF + ((m * 90 + wf) * 64 + lane) * 16, 0, 16);
            const float cs = xg_sum(dot2bf(fr[0], 0x3F803F80u, dot2bf(fr[1], 0x3F803F80u, dot2bf(fr[2], 0x3F803F80u, dot2bf(fr[3], 0x3F803F80u, 0.f)))));
            __builtin_amdgcn_raw_buffer_store_b32(__float_as_uint(cs), rws, lane < 16 ? (unsigned)WS_CSP + ((m * 90 + wf) * 16 + lane) * 4 : OOB, 0, 16); }
        return;
    }
    const int T = g * 64 + rt * 16 + r16;
    const u16* xsrc = p.x + (size_t)T * D + kh * 288 + gq * 8;
    u32x4 xa[9];
    asm volatile("global_load_dwordx4 %0, %9, off\n\tglobal_load_dwordx4 %1, %9, off offset:64\n\tglobal_load_dwordx4 %2, %9, off offset:128\n\t"
                 "global_load_dwordx4 %3, %9, off offset:192\n\tglobal_load_dwordx4 %4, %9, off offset:256\n\tglobal_load_dwordx4 %5, %9, off offset:320\n\t"
                 "global_load_dwordx4 %6, %9, off offset:384\n\tglobal_load_dwordx4 %7, %9, off offset:448\n\tglobal_load_dwordx4 %8, %9, off offset:512"
                 : "=&v"(xa[0]), "=&v"(xa[1]), "=&v"(xa[2]), "=&v"(xa[3]), "=&v"(xa[4]), "=&v"(xa[5]), "=&v"(xa[6]), "=&v"(xa[7]), "=&v"(xa[8]) : "v"(xsrc) : "memory");
    { const char* qsrc = (const char*)p.wq + 144 * m;
      int prow = (tid * 7282) >> 16, pc = tid - prow * 9;
#pragma unroll
      for (int i = 0; i < 11; ++i) { if (i < 10 || wave == 0) {
          const int k = (prow & ~12) | ((prow & 4) << 1) | ((prow & 8) >> 1);
          glds16s(qsrc, k * 1152 + 16 * pc, __builtin_amdgcn_readfirstlane(lds0 + (i * 512 + wave * 64) * 16)); }
          const bool wrap = pc >= 1; pc = wrap ? pc - 1 : pc + 8; prow += wrap ? 57 : 56; }
      const char* kvsrc = (const char*)((m == 2 || m == 3 || m == 5) ? p.wv : p.wk) + ((m == 1 || m == 3) ? 16 : (m == 4 || m == 5) ? 20 : 0);
      { const int pr = tid, k = (pr & ~12) | ((pr & 4) << 1) | ((pr & 8) >> 1); glds16s(kvsrc, k * 36, __builtin_amdgcn_readfirstlane(lds0 + WIMG + wave * 1024)); }
      if (wave == 1) { const int pr = 512 + lane, k = (pr & ~12) | ((pr & 4) << 1) | ((pr & 8) >> 1); glds16s(kvsrc, k * 36, __builtin_amdgcn_readfirstlane(lds0 + WIMG + 8192)); } }
    STAMP(0, 1);
    QkvOff qo = qkv_offsets(WS_QF, WS_KV, b, m, T, kh, gq); PIN_QKV(qo);
    asm volatile("s_waitcnt vmcnt(11)" : "+v"(xa[0]), "+v"(xa[1]), "+v"(xa[2]), "+v"(xa[3]), "+v"(xa[4]), "+v"(xa[5]), "+v"(xa[6]), "+v"(xa[7]), "+v"(xa[8]) :: "memory");
    float sum, sq; ln_sums(xa, sum, sq);
    LAS f32x2* st = (LAS f32x2*)(lds + WIMG + KVIMG);
    LAS unsigned* cnt = (LAS unsigned*)(lds + WIMG + KVIMG + 1024);
    if (tid == 0) *cnt = 0;
    if (gq == 0) st[wave * 16 + r16] = f32x2{sum, sq};
    STAMP(0, 2);
    BAR_ALL();
    STAMP(0, 3);
    { const f32x2 o = st[(wave ^ 4) * 16 + r16]; sum += o[0]; sq += o[1]; }
    const float mu = sum * (1.f / D), var = fmaxf(sq * (1.f / D) - mu * mu, 0.f), rs = rsqrtf(var + 1e-5f);
    bf16x8 xf[9]; ln_apply(xa, mu, rs, xf);
    STAMP(0, 4);
    LAS const char* wb = (LAS const char*)lds + (kh * 288 + prow0) * WROW + (r16 & 3) * 8;
    LAS const char* wb4 = (r16 & 3) < 2 ? wb + 128 : (LAS const char*)lds + WIMG + (kh * 288 + prow0) * 16 + ((r16 & 3) - 2) * 8;
    const int st4 = (r16 & 3) < 2 ? WROW : 16;
    auto ldw = [&](int s, int ct) { const s16x4 lo = ct < 4 ? trread(wb + s * 32 * WROW + ct * 32) : trread(wb4 + s * 32 * st4),
                                                hi = ct < 4 ? trread(wb + s * 32 * WROW + 8 * WROW + ct * 32) : trread(wb4 + (s * 32 + 8) * st4);
        return __builtin_bit_cast(bf16x8, __builtin_shufflevector(lo, hi, 0, 1, 2, 3, 4, 5, 6, 7)); };
    f32x4 acc[5] = {}, o3[3];
    gemm45(acc, xf, ldw);
    STAMP(0, 5);
    khalf_reduce<true>((LAS f32x4*)(lds + WIMG + KVIMG + 4096), acc, o3, rt, kh, lane, cnt);
    store_qkv3(rws, qo, WS_KV, m, g, tid, o3);
    STAMP(0, 6);
}

enum { W_ASYNC = 0, W_HSYNC = 1, W_READY = 2, W_SEL = 3, W_OZBAD = 4, W_WSUM = 5, W_OZDONE = 6, W_ISSUED = 7, W_BAD = 8, W_HLAND = 16 };
struct FragQ { bf16x8 a1, a2, b1, b2; };
#define ATT_LOOP_ASM \
    "1:\n\t" \
    "v_smfmac_f32_32x32x32_bf16 v[16:31], v[124:127], v[88:95], v128\n\t" \
    "v_exp_f32 v32, v32\n\t" \
    "v_exp_f32 v33, v33\n\t" \
    "v_exp_f32 v34, v34\n\t" \
    "v_exp_f32 v35, v35\n\t" \
    "s_waitcnt lgkmcnt(3)\n\t" \
    "v_mfma_f32_32x32x4bf16_1k v[64:95], v[112:113], v[104:105], 0\n\t" \
    "v_exp_f32 v36, v36\n\t" \
    "v_exp_f32 v37, v37\n\t" \
    "v_exp_f32 v38, v38\n\t" \
    "v_exp_f32 v39, v39\n\t" \
    "v_exp_f32 v40, v40\n\t" \
    "v_exp_f32 v41, v41\n\t" \
    "v_exp_f32 v42, v42\n\t" \
    "v_exp_f32 v43, v43\n\t" \
    "s_waitcnt lgkmcnt(1)\n\t" \
    "v_smfmac_f32_32x32x32_bf16 v[0:15], v[116:119], v[32:39], v128\n\t" \
    "v_exp_f32 v44, v44\n\t" \
    "v_exp_f32 v45, v45\n\t" \
    "v_exp_f32 v46, v46\n\t" \
    "v_exp_f32 v47, v47\n\t" \
    "ds_read_b32 v112, v130 offset:9728\n\t" \
    "s_waitcnt lgkmcnt(1)\n\t" \
    "v_smfmac_f32_32x32x32_bf16 v[0:15], v[120:123], v[40:47], v128\n\t" \
    "v_exp_f32 v48, v48\n\t" \
    "v_exp_f32 v49, v49\n\t" \
    "v_exp_f32 v50, v50\n\t" \
    "v_exp_f32 v51, v51\n\t" \
    "v_mfma_f32_32x32x16_bf16 v[64:79], v[108:111], v[96:99], v[64:79]\n\t" \
    "v_exp_f32 v52, v52\n\t" \
    "v_exp_f32 v53, v53\n\t" \
    "v_exp_f32 v54, v54\n\t" \
    "v_exp_f32 v55, v55\n\t" \
    "v_mfma_f32_32x32x16_bf16 v[80:95], v[108:111], v[100:103], v[80:95]\n\t" \
    "ds_read_b128 v[108:111], v129 offset:9728\n\t" \
    "v_exp_f32 v56, v56\n\t" \
    "v_exp_f32 v57, v57\n\t" \
    "v_exp_f32 v58, v58\n\t" \
    "v_exp_f32 v59, v59\n\t" \
    "v_smfmac_f32_32x32x32_bf16 v[16:31], v[116:119], v[48:55], v128\n\t" \
    "v_exp_f32 v60, v60\n\t" \
    "v_exp_f32 v61, v61\n\t" \
    "v_exp_f32 v62, v62\n\t" \
    "v_exp_f32 v63, v63\n\t" \
    "ds_read_b128 v[116:119], v131 offset:4864\n\t" \
    "ds_read_b128 v[124:127], v131 offset:5504\n\t" \
    "s_nop 0\n\t" \
    "v_smfmac_f32_32x32x32_bf16 v[16:31], v[120:123], v[56:63], v128\n\t" \
    "v_exp_f32 v64, v64\n\t" \
    "v_exp_f32 v65, v65\n\t" \
    "v_exp_f32 v66, v66\n\t" \
    "v_exp_f32 v67, v67\n\t" \
    "s_waitcnt lgkmcnt(3)\n\t" \
    "v_mfma_f32_32x32x4bf16_1k v[32:63], v[112:113], v[104:105], 0\n\t" \
    "v_exp_f32 v68, v68\n\t" \
    "v_exp_f32 v69, v69\n\t" \
    "v_exp_f32 v70, v70\n\t" \
    "v_exp_f32 v71, v71\n\t" \
    "v_exp_f32 v72, v72\n\t" \
    "v_exp_f32 v73, v73\n\t" \
    "v_exp_f32 v74, v74\n\t" \
    "v_exp_f32 v75, v75\n\t" \
    "s_waitcnt lgkmcnt(1)\n\t" \
    "v_smfmac_f32_32x32x32_bf16 v[0:15], v[116:119], v[64:71], v128\n\t" \
    "v_exp_f32 v76, v76\n\t" \
    "v_exp_f32 v77, v77\n\t" \
    "v_exp_f32 v78, v78\n\t" \
    "v_exp_f32 v79, v79\n\t" \
    "ds_read_b32 v112, v130 offset:14592\n\t" \
    "s_waitcnt lgkmcnt(1)\n\t" \
    "v_smfmac_f32_32x32x32_bf16 v[0:15], v[124:127], v[72:79], v128\n\t" \
    "v_exp_f32 v80, v80\n\t" \
    "v_exp_f32 v81, v81\n\t" \
    "v_exp_f32 v82, v82\n\t" \
    "v_exp_f32 v83, v83\n\t" \
    "v_mfma_f32_32x32x16_bf16 v[32:47], v[108:111], v[96:99], v[32:47]\n\t" \
    "v_exp_f32 v84, v84\n\t" \
    "v_exp_f32 v85, v85\n\t" \
    "v_exp_f32 v86, v86\n\t" \
    "v_exp_f32 v87, v87\n\t" \
    "v_mfma_f32_32x32x16_bf16 v[48:63], v[108:111], v[100:103], v[48:63]\n\t" \
    "ds_read_b128 v[108:111], v129 offset:14592\n\t" \
    "v_exp_f32 v88, v88\n\t" \
    "v_exp_f32 v89, v89\n\t" \
    "v_exp_f32 v90, v90\n\t" \
    "v_exp_f32 v91, v91\n\t" \
    "v_smfmac_f32_32x32x32_bf16 v[16:31], v[116:119], v[80:87], v128\n\t" \
    "v_exp_f32 v92, v92\n\t" \
    "v_exp_f32 v93, v93\n\t" \
    "v_exp_f32 v94, v94\n\t" \
    "v_exp_f32 v95, v95\n\t" \
    "ds_read_b128 v[116:119], v131 offset:9728\n\t" \
    "ds_read_b128 v[120:123], v131 offset:10368\n\t" \
    "s_nop 0\n\t" \
    "v_smfmac_f32_32x32x32_bf16 v[16:31], v[124:127], v[88:95], v128\n\t" \
    "v_exp_f32 v32, v32\n\t" \
    "v_exp_f32 v33, v33\n\t" \
    "v_exp_f32 v34, v34\n\t" \
    "v_exp_f32 v35, v35\n\t" \
    "s_waitcnt lgkmcnt(3)\n\t" \
    "v_mfma_f32_32x32x4bf16_1k v[64:95], v[112:113], v[104:105], 0\n\t" \
    "v_exp_f32 v36, v36\n\t" \
    "v_exp_f32 v37, v37\n\t" \
    "v_exp_f32 v38, v38\n\t" \
    "v_exp_f32 v39, v39\n\t" \
    "v_exp_f32 v40, v40\n\t" \
    "v_exp_f32 v41, v41\n\t" \
    "v_exp_f32 v42, v42\n\t" \
    "v_exp_f32 v43, v43\n\t" \
    "s_waitcnt lgkmcnt(1)\n\t" \
    "v_smfmac_f32_32x32x32_bf16 v[0:15], v[116:119], v[32:39], v128\n\t" \
    "v_exp_f32 v44, v44\n\t" \
    "v_exp_f32 v45, v45\n\t" \
    "v_exp_f32 v46, v46\n\t" \
    "v_exp_f32 v47, v47\n\t" \
    "ds_read_b32 v112, v130 offset:19456\n\t" \
    "s_waitcnt lgkmcnt(1)\n\t" \
    "v_smfmac_f32_32x32x32_bf16 v[0:15], v[120:123], v[40:47], v128\n\t" \
    "v_exp_f32 v48, v48\n\t" \
    "v_exp_f32 v49, v49\n\t" \
    "v_exp_f32 v50, v50\n\t" \
    "v_exp_f32 v51, v51\n\t" \
    "v_mfma_f32_32x32x16_bf16 v[64:79], v[108:111], v[96:99], v[64:79]\n\t" \
    "v_exp_f32 v52, v52\n\t" \
    "v_exp_f32 v53, v53\n\t" \
    "v_exp_f32 v54, v54\n\t" \
    "v_exp_f32 v55, v55\n\t" \
    "v_mfma_f32_32x32x16_bf16 v[80:95], v[108:111], v[100:103], v[80:95]\n\t" \
    "ds_read_b128 v[108:111], v129 offset:19456\n\t" \
    "v_exp_f32 v56, v56\n\t" \
    "v_exp_f32 v57, v57\n\t" \
    "v_exp_f32 v58, v58\n\t" \
    "v_exp_f32 v59, v59\n\t" \
    "v_smfmac_f32_32x32x32_bf16 v[16:31], v[116:119], v[48:55], v128\n\t" \
    "v_exp_f32 v60, v60\n\t" \
    "v_exp_f32 v61, v61\n\t" \
    "v_exp_f32 v62, v62\n\t" \
    "v_exp_f32 v63, v63\n\t" \
    "ds_read_b128 v[116:119], v131 offset:14592\n\t" \
    "ds_read_b128 v[124:127], v131 offset:15232\n\t" \
    "s_nop 0\n\t" \
    "v_smfmac_f32_32x32x32_bf16 v[16:31], v[120:123], v[56:63], v128\n\t" \
    "v_exp_f32 v64, v64\n\t" \
    "v_exp_f32 v65, v65\n\t" \
    "v_exp_f32 v66, v66\n\t" \
    "v_exp_f32 v67, v67\n\t" \
    "s_waitcnt lgkmcnt(3)\n\t" \
    "v_mfma_f32_32x32x4bf16_1k v[32:63], v[112:113], v[104:105], 0\n\t" \
    "v_exp_f32 v68, v68\n\t" \
    "v_exp_f32 v69, v69\n\t" \
    "v_exp_f32 v70, v70\n\t" \
    "v_exp_f32 v71, v71\n\t" \
    "v_exp_f32 v72, v72\n\t" \
    "v_exp_f32 v73, v73\n\t" \
    "v_exp_f32 v74, v74\n\t" \
    "v_exp_f32 v75, v75\n\t" \
    "s_waitcnt lgkmcnt(1)\n\t" \
    "v_smfmac_f32_32x32x32_bf16 v[0:15], v[116:119], v[64:71], v128\n\t" \
    "v_exp_f32 v76, v76\n\t" \
    "v_exp_f32 v77, v77\n\t" \
    "v_exp_f32 v78, v78\n\t" \
    "v_exp_f32 v79, v79\n\t" \
    "ds_read_b32 v112, v130 offset:24320\n\t" \
    "s_waitcnt lgkmcnt(1)\n\t" \
    "v_smfmac_f32_32x32x32_bf16 v[0:15], v[124:127], v[72:79], v128\n\t" \
    "v_exp_f32 v80, v80\n\t" \
    "v_exp_f32 v81, v81\n\t" \
    "v_exp_f32 v82, v82\n\t" \
    "v_exp_f32 v83, v83\n\t" \
    "v_mfma_f32_32x32x16_bf16 v[32:47], v[108:111], v[96:99], v[32:47]\n\t" \
    "v_exp_f32 v84, v84\n\t" \
    "v_exp_f32 v85, v85\n\t" \
    "v_exp_f32 v86, v86\n\t" \
    "v_exp_f32 v87, v87\n\t" \
    "v_mfma_f32_32x32x16_bf16 v[48:63], v[108:111], v[100:103], v[48:63]\n\t" \
    "ds_read_b128 v[108:111], v129 offset:24320\n\t" \
    "v_exp_f32 v88, v88\n\t" \
    "v_exp_f32 v89, v89\n\t" \
    "v_exp_f32 v90, v90\n\t" \
    "v_exp_f32 v91, v91\n\t" \
    "v_smfmac_f32_32x32x32_bf16 v[16:31], v[116:119], v[80:87], v128\n\t" \
    "v_exp_f32 v92, v92\n\t" \
    "v_exp_f32 v93, v93\n\t" \
    "v_exp_f32 v94, v94\n\t" \
    "v_exp_f32 v95, v95\n\t" \
    "ds_read_b128 v[116:119], v131 offset:19456\n\t" \
    "ds_read_b128 v[120:123], v131 offset:20096\n\t" \
    "v_add_u32 v129, 0x4c00, v129\n\t" \
    "v_add_u32 v130, 0x4c00, v130\n\t" \
    "v_add_u32 v131, 0x4c00, v131\n\t" \
    "s_sub_u32 %[n], %[n], 1\n\t" \
    "s_cmp_lg_u32 %[n], 0\n\t" \
    "s_cbranch_scc1 1b\n\t" \
    ""
#define ATT_RUN(N) do { unsigned n_ = (N); \
    asm volatile("s_nop 1\n\t" ATT_LOOP_ASM "s_waitcnt lgkmcnt(0)\n\ts_nop 7\n\ts_nop 7" \
        : "+{v[0:15]}"(oa), "+{v[16:31]}"(ob), "+{v[32:63]}"(s0), "+{v[64:95]}"(s1), \
          "+{v[108:111]}"(k1), "+{v[112:113]}"(k2u), "+{v[116:119]}"(v0), "+{v[120:123]}"(v1a), "+{v[124:127]}"(v1b), \
          "+{v129}"(kp), "+{v130}"(k2p), "+{v131}"(vp), [n] "+s"(n_) \
        : "{v[96:99]}"(q.a1), "{v[100:103]}"(q.b1), "{v[104:105]}"(q2), "{v128}"(0xDDDDDDDDu) : "scc", "memory"); } while (0)
typedef float f32x32 __attribute__((ext_vector_type(32)));
DI void attn_pass(LAS const char* lds, int wave, int lane, const FragQ& q, f32x16& oa, f32x16& ob, LAS unsigned* cnt) {
    const int r = lane & 31, h = lane >> 5, kh = wave >> 2;
    const int dsel = r < 19 ? r : 19;
    const unsigned l0 = (unsigned)(uintptr_t)lds + kh * TILE_B;
    unsigned kp = l0 + lane * 16, k2p = l0 + K2_OFF + r * 4, vp = l0 + V_OFF + h * VSUB + dsel * 16;
    const f32x16 zero = {};
    u32x2 q2; { const unsigned qa = __builtin_bit_cast(u32x4, q.a2)[0], qb = __builtin_bit_cast(u32x4, q.b2)[0];
        auto sw = __builtin_amdgcn_permlane32_swap(qa, qb, false, false); q2 = u32x2{sw[0], 0u}; }
    bf16x8 k1 = *(LAS const bf16x8*)(uintptr_t)kp; u32x4 k2f = {*(LAS const unsigned*)(uintptr_t)k2p, 0, 0, 0};
    f32x16 sa0, sb0;
    sa0 = MFMA32(k1, q.a1, zero); sa0 = MFMA32(__builtin_bit_cast(bf16x8, k2f), q.a2, sa0);
    sb0 = MFMA32(k1, q.b1, zero); sb0 = MFMA32(__builtin_bit_cast(bf16x8, k2f), q.b2, sb0);
    f32x32 s0 = __builtin_shufflevector(sa0, sb0, 0, 1, 2, 3, 4, 5, 6, 7, 8, 9, 10, 11, 12, 13, 14, 15, 16, 17, 18, 19, 20, 21, 22, 23, 24, 25, 26, 27, 28, 29, 30, 31), s1 = {};
    k1 = *(LAS const bf16x8*)(uintptr_t)(kp + 2 * TILE_B); u32x2 k2u = {*(LAS const unsigned*)(uintptr_t)(k2p + 2 * TILE_B), 0u};
    bf16x8 v0 = *(LAS const bf16x8*)(uintptr_t)vp, v1a = *(LAS const bf16x8*)(uintptr_t)(vp + 2 * VSUB), v1b = {};
    if (kh == 1) __builtin_amdgcn_s_setprio(1);
    asm volatile("s_nop 7\n\ts_nop 7" : "+v"(s0));
    ATT_RUN(2);
    while (__builtin_amdgcn_readfirstlane(__hip_atomic_load(cnt + (W_HLAND - W_ASYNC), __ATOMIC_RELAXED, __HIP_MEMORY_SCOPE_WORKGROUP)) < 4u) __builtin_amdgcn_s_sleep(1);
    lds_sync(cnt, 16u, lane);
    ATT_RUN(4);
    __builtin_amdgcn_s_setprio(0);
    ATT_RUN(2);
    ob = pv(v1b, __builtin_shufflevector(s1, s1, 24, 25, 26, 27, 28, 29, 30, 31), ob);
}
DI void stage_rest(const char* kvg, unsigned lds0, int hw, int lane) {
    unsigned sv, voff = 49152 + hw * 1024 + lane * 16; const unsigned dst = __builtin_amdgcn_readfirstlane(lds0 + 49152 + hw * 1024);
    asm volatile("s_mov_b32 %0, m0\n\ts_mov_b32 m0, %3\n\ts_nop 0\n\t.rept 26\n\tglobal_load_lds_dwordx4 %1, %2\n\ts_add_u32 m0, m0, 0x1000\n\tv_add_u32 %1, 0x1000, %1\n\t.endr\n\ts_mov_b32 m0, %0"
                 : "=&s"(sv), "+v"(voff) : "s"(kvg), "s"(dst) : "memory", "scc");
}
DI u32x2 stage_rest_ld(const char* kvg, unsigned lds0, int hw, int lane, const char* src) {
    unsigned sv, voff = 49152 + hw * 1024 + lane * 16; const unsigned dst = __builtin_amdgcn_readfirstlane(lds0 + 49152 + hw * 1024); u32x2 v;
    asm volatile("global_load_dwordx2 %0, %5, off\n\ts_mov_b32 %1, m0\n\ts_mov_b32 m0, %4\n\ts_nop 0\n\t.rept 26\n\tglobal_load_lds_dwordx4 %2, %3\n\ts_add_u32 m0, m0, 0x1000\n\tv_add_u32 %2, 0x1000, %2\n\t.endr\n\ts_mov_b32 m0, %1\n\ts_waitcnt vmcnt(26)"
                 : "=&v"(v), "=&s"(sv), "+v"(voff) : "s"(kvg), "s"(dst), "v"(src) : "memory", "scc");
    return v;
}
template <int MODE>
DI void attn_slow(LAS const char* lds, int t0, int dt, int n, int lane, const FragQ& q, float& ma, float& mb, f32x16& oa, f32x16& ob) {
    const int r = lane & 31, h = lane >> 5, dsel = r < 19 ? r : 19; const f32x16 zero = {};
    if (MODE == 0) { ma = -INFINITY; mb = -INFINITY; }
    for (int i = 0; i < n; ++i) { LAS const char* t = lds + (t0 + i * dt) * TILE_B;
        const bf16x8 k1 = *(LAS const bf16x8*)(t + lane * 16); u32x4 k2u = {*(LAS const unsigned*)(t + K2_OFF + r * 4), 0, 0, 0};
        f32x16 sa = MFMA32(k1, q.a1, zero); sa = MFMA32(__builtin_bit_cast(bf16x8, k2u), q.a2, sa);
        if (MODE == 2) { const bf16x8 v0 = *(LAS const bf16x8*)(t + V_OFF + h * VSUB + dsel * 16), v1 = *(LAS const bf16x8*)(t + V_OFF + (2 + h) * VSUB + dsel * 16);
            for (int j = 0; j < 16; ++j) sa[j] = __builtin_amdgcn_exp2f(sa[j]);
            oa = pv(v0, lo8(sa), oa); oa = pv(v1, hi8(sa), oa); continue; }
        f32x16 sb = MFMA32(k1, q.b1, zero); sb = MFMA32(__builtin_bit_cast(bf16x8, k2u), q.b2, sb);
        if (MODE == 0) { for (int j = 0; j < 16; ++j) { ma = fmaxf(ma, sa[j]); mb = fmaxf(mb, sb[j]); } }
        else { const bf16x8 v0 = *(LAS const bf16x8*)(t + V_OFF + h * VSUB + dsel * 16), v1 = *(LAS const bf16x8*)(t + V_OFF + (2 + h) * VSUB + dsel * 16);
            for (int j = 0; j < 16; ++j) { sa[j] = __builtin_amdgcn_exp2f(sa[j] - ma); sb[j] = __builtin_amdgcn_exp2f(sb[j] - mb); }
            oa = pv(v0, lo8(sa), oa); oa = pv(v1, hi8(sa), oa); ob = pv(v0, lo8(sb), ob); ob = pv(v1, hi8(sb), ob); } }
    if (MODE == 0) { ma = fmaxf(ma, __shfl_xor(ma, 32)); mb = fmaxf(mb, __shfl_xor(mb, 32)); }
}
DI bool o_bad(const f32x16& o, int h) {
    bool bad = false;
    if (h == 0) { const float l = o[10]; bad = !(l >= 0x1p-60f && l <= 0x1p59f); }
    for (int j = 0; j < 10; ++j) bad |= !(fabsf(o[j]) < INFINITY);
    return bad;
}
DI void wait_flags8(GAS unsigned* flags, unsigned epoch, int lane, LAS unsigned* slot) {
    const char* a = (const char*)(flags + (lane & 7) * 64); const unsigned dst = __builtin_amdgcn_readfirstlane((unsigned)(uintptr_t)slot);
    auto issue = [&](int k) { unsigned sv; asm volatile("s_mov_b32 %0, m0\n\ts_mov_b32 m0, %2\n\ts_nop 0\n\tglobal_load_lds_dword %1, off sc1\n\ts_mov_b32 m0, %0" : "=&s"(sv) : "v"(a), "s"(dst + k * 256) : "memory"); };
    issue(0); __builtin_amdgcn_s_sleep(8); issue(1); __builtin_amdgcn_s_sleep(8); issue(2);
    for (unsigned sp = 0; sp < (1u << 21); ++sp) {
        asm volatile("s_waitcnt vmcnt(2)" ::: "memory"); if (__all(slot[lane] == epoch)) break; asm volatile("" ::: "memory"); issue(0);
        asm volatile("s_waitcnt vmcnt(2)" ::: "memory"); if (__all(slot[64 + lane] == epoch)) break; asm volatile("" ::: "memory"); issue(1);
        asm volatile("s_waitcnt vmcnt(2)" ::: "memory"); if (__all(slot[128 + lane] == epoch)) break; asm volatile("" ::: "memory"); issue(2);
    }
    asm volatile("" ::: "memory");
}
DI void put_granule(char* ws, unsigned off, unsigned v) { __hip_atomic_store((GAS unsigned long long*)(ws + off), (1ull << 32) | v, RLX_AGENT); }
DI unsigned long long ld_granule(char* ws, unsigned off) { return __hip_atomic_load((GAS unsigned long long*)(ws + off), RLX_AGENT); }
template <int N> DI void get_granules(char* ws, unsigned off, unsigned stride, bool act, unsigned (&v)[N], bool nap) {
    unsigned sp = 0;
    for (;;) { bool ok = true;
#pragma unroll
        for (int k = 0; k < N; ++k) { const unsigned long long x = act ? ld_granule(ws, off + k * stride) : (1ull << 32); v[k] = (unsigned)x; ok &= (x >> 32) != 0; }
        if (__all(ok) || ++sp > (1u << 20)) break;
        if (nap) __builtin_amdgcn_s_sleep(4); }
}

constexpr int ATT_LDS = 160 * 1024;
constexpr int B1_OFF = 32768;
constexpr int SP = KV_B;
constexpr int SP_W = SP, SP_T = SP + 128, SP_XS = SP + 1152, SP_QN = SP + 3456, SP_KVN = SP + 3776, SP_OZ = SP + 3936, SP_PZ = SP + 4096, SP_SCR = SP + 5280;
constexpr int SP_SCR2 = SP + 6304;
constexpr int SP_RED = SP + 2432, SP_HS = SP + 2688;

struct AttnCtx { int tid, lane, wave, r, h, pp, kh, b, g, m, rt, r16, gq, T2; unsigned lds0; };
DI AttnCtx attn_ctx(char* lds) { AttnCtx c; c.tid = threadIdx.x; c.lane = c.tid & 63; c.r = c.lane & 31; c.h = c.lane >> 5; c.wave = __builtin_amdgcn_readfirstlane(c.tid >> 6);
    c.pp = c.wave & 3; c.kh = (c.wave >> 2) & 1; c.b = blockIdx.x; c.g = blk_group(c.b); c.m = blk_member(c.b); c.rt = c.wave & 3; c.r16 = c.lane & 15; c.gq = c.lane >> 4;
    c.T2 = c.g * 64 + c.rt * 16 + c.r16; c.lds0 = (unsigned)(uintptr_t)lds; return c; }
DI void load_q_stage1(const AttnCtx& c, const char* qb, const char* kv, FragQ& q, LAS unsigned* W) {
    u32x4 qa1, qb1; unsigned qa2r, qb2r;
    asm volatile("global_load_dwordx4 %0, %4, off\n\tglobal_load_dwordx4 %1, %4, off offset:1152\n\tglobal_load_dword %2, %5, off offset:1024\n\tglobal_load_dword %3, %5, off offset:2176"
                 : "=&v"(qa1), "=&v"(qb1), "=&v"(qa2r), "=&v"(qb2r) : "v"(qb + c.lane * 16), "v"(qb + c.r * 4) : "memory");
#pragma unroll
    for (int i = 0; i < 6; ++i) glds16s(kv + i * 8192, c.wave * 1024 + c.lane * 16, __builtin_amdgcn_readfirstlane(c.lds0 + c.wave * 1024) + i * 8192);
    if (c.lane == 0) __hip_atomic_fetch_add(W + W_ISSUED, 1u, __ATOMIC_RELAXED, __HIP_MEMORY_SCOPE_WORKGROUP);
    asm volatile("s_waitcnt vmcnt(0)" : "+v"(qa1), "+v"(qb1), "+v"(qa2r), "+v"(qb2r) :: "memory");
    q.a1 = __builtin_bit_cast(bf16x8, qa1); q.b1 = __builtin_bit_cast(bf16x8, qb1);
    u32x4 t; t[0] = c.h ? 0u : qa2r; t[1] = 0; t[2] = 0; t[3] = 0; q.a2 = __builtin_bit_cast(bf16x8, t); t[0] = c.h ? 0u : qb2r; q.b2 = __builtin_bit_cast(bf16x8, t);
}
DI LAS f32x4* exbuf(char* lds, int w) { return (LAS f32x4*)(lds + w * 3072); }
DI void put_partial(char* lds, const AttnCtx& c, const f32x16& oa, const f32x16& ob) { LAS f32x4* ex = exbuf(lds, c.wave); const f32x16& snd = c.kh ? oa : ob;
    ex[c.lane] = f32x4{snd[0], snd[1], snd[2], snd[3]}; ex[64 + c.lane] = f32x4{snd[4], snd[5], snd[6], snd[7]}; ex[128 + c.lane] = f32x4{snd[8], snd[9], snd[10], snd[11]}; }
DI bool any_bad(char* lds) { LAS const u32x4* f = (LAS const u32x4*)(lds + SP_W + W_BAD * 4); const u32x4 f0 = f[0], f1 = f[1];
    return __builtin_amdgcn_readfirstlane(f0[0] | f0[1] | f0[2] | f0[3] | f1[0] | f1[1] | f1[2] | f1[3]) != 0; }
DI void fallback_restore(const AttnCtx& c, const char* kv, bool loader) {
    BAR_LDS();
    if (loader) {
#pragma unroll
        for (int i = 0; i < 3; ++i) glds16s(kv + i * 8192, c.wave * 1024 + c.lane * 16, __builtin_amdgcn_readfirstlane(c.lds0 + c.wave * 1024) + i * 8192); }
    BAR_ALL();
}
DI void fallback_pass(char* lds, const AttnCtx& c, const FragQ& q, f32x16& oa, f32x16& ob) {
    LAS float* scr = (LAS float*)(lds + SP_SCR);
    float ma, mb; attn_slow<0>((LAS const char*)lds, c.kh, 2, 32, c.lane, q, ma, mb, oa, ob);
    if (c.h == 0) { scr[(c.wave * 2) * 32 + c.r] = ma; scr[(c.wave * 2 + 1) * 32 + c.r] = mb; }
    BAR_LDS();
    ma = fmaxf(ma, scr[((c.wave ^ 4) * 2) * 32 + c.r]); mb = fmaxf(mb, scr[((c.wave ^ 4) * 2 + 1) * 32 + c.r]);
    oa = f32x16{}; ob = f32x16{};
    attn_slow<1>((LAS const char*)lds, c.kh, 2, 32, c.lane, q, ma, mb, oa, ob);
    BAR_LDS();
    put_partial(lds, c, oa, ob);
    BAR_LDS();
}
DI void finish_o(char* lds, const AttnCtx& c, const f32x16& oa, const f32x16& ob, f32x4& a, f32x4& e, f32x2& z) {
    LAS const f32x4* rx = exbuf(lds, c.wave ^ 4); const f32x16& own = c.kh ? ob : oa;
    a = rx[c.lane] + f32x4{own[0], own[1], own[2], own[3]}; e = rx[64 + c.lane] + f32x4{own[4], own[5], own[6], own[7]};
    const f32x4 t = rx[128 + c.lane] + f32x4{own[8], own[9], own[10], own[11]};
    const float il = __builtin_amdgcn_rcpf(lo32_bcast(t[2]));
    a *= il; e *= il; z = f32x2{t[0] * il, t[1] * il};
}
DI void publish_o(rsrc_t rws, unsigned xo, const AttnCtx& c, const f32x4& a, const f32x4& e, const f32x2& z) {
    u32x4 v = {nopend(pk2(a[0], a[1])), nopend(pk2(a[2], a[3])), nopend(pk2(e[0], e[1])), nopend(pk2(e[2], e[3]))};
    __builtin_amdgcn_raw_buffer_store_b128(v, rws, xo + ((9 * c.m + 2 * c.pp + c.h) * 64 + 32 * c.kh + c.r) * 16, 0, 16);
    __builtin_amdgcn_raw_buffer_store_b32(nopend(pk2(z[0], z[1])), rws, c.h == 0 ? xo + ((9 * c.m + 8) * 64 + 32 * c.kh + c.r) * 16 + c.pp * 4 : OOB, 0, 16);
}
DI void stage_frags(const AttnCtx& c, const char* src, int w, int nw) {
    for (int f = w; f < 90; f += nw) glds16s(src + f * 1024, c.lane * 16, __builtin_amdgcn_readfirstlane(c.lds0 + B1_OFF + f * 1024));
}

__global__ __launch_bounds__(768) void k_attn0(Params p) {
    extern __shared__ __attribute__((aligned(16))) char lds[];
    const AttnCtx c = attn_ctx(lds);
    const int tid = c.tid, lane = c.lane, wave = c.wave, kh = c.kh, m = c.m, g = c.g, rt = c.rt, r16 = c.r16, gq = c.gq, T2 = c.T2;
    STAMP_DECL; STAMP(1, 0);
    LAS unsigned* W = (LAS unsigned*)(lds + SP_W);
    if (tid < 32) W[tid] = 0;
    BAR_LDS();
    const char* kv = p.ws + WS_KV;
    const rsrc_t rws = mkrs(p.ws, WS_END);
    GAS unsigned* flags = (GAS unsigned*)(p.ws + WS_FLAG) + (size_t)g * 8 * 64;
    LAS float* cs = (LAS float*)(lds + 28672);
    if (wave >= 8) {
        const int hw = wave - 8;
        unsigned cv[18];
        { const int n = hw * 64 + lane, nn = n < 80 ? n : 0;
#pragma unroll
          for (int Sx = 0; Sx < 18; ++Sx) cv[Sx] = __builtin_amdgcn_raw_buffer_load_b32(hw < 2 ? rws : mkrs(p.wr, D * 2), hw < 2 ? (unsigned)WS_CSP + ((m * 90 + (nn >> 4)) * 16 + (nn & 15)) * 4 : lane * 4, hw < 2 ? Sx * 5 * 64 : Sx * 256, 0); }
        while (__builtin_amdgcn_readfirstlane(__hip_atomic_load(W + W_ISSUED, __ATOMIC_RELAXED, __HIP_MEMORY_SCOPE_WORKGROUP)) < 8u) __builtin_amdgcn_s_sleep(1);
        stage_rest(kv, c.lds0, hw, lane);
        const bool isF = (c.b >> 3) < 18;
        if (!isF) { asm volatile("s_waitcnt vmcnt(0)" ::: "memory"); if (lane == 0) __hip_atomic_fetch_add(W + W_HLAND, 1u, __ATOMIC_RELAXED, __HIP_MEMORY_SCOPE_WORKGROUP);
            constexpr unsigned N16 = 32 * XO_G / 16; const unsigned e0 = (((c.b >> 3) - 18) * 8 + (c.b & 7)) * 256 + (tid - 512);
#pragma unroll
            for (unsigned i = 0; i < (N16 + 28671) / 28672; ++i) { const unsigned e = i * 28672 + e0;
                __builtin_amdgcn_raw_buffer_store_b128(u32x4{PENDING, PENDING, PENDING, PENDING}, rws, e < N16 ? (unsigned)WS_XO1 + e * 16 : OOB, 0, 16); } }
        STAMP(1, 1);
        if (isF) {
            const int fc = (c.b & 7) * 18 + (c.b >> 3);
            const rsrc_t rg_ = mkrs(p.wg, D * F * 2), ru_ = mkrs(p.wu, D * F * 2), rd_ = mkrs(p.wd, D * F * 2);
            const unsigned vgu = (lane >> 1) * (F * 2) + fc * 64 + (lane & 1) * 16, vd = (fc * 32 + (lane >> 1)) * (D * 2) + (lane & 1) * 16;
            u32x4 u[27];
#pragma unroll
            for (int i = 0; i < 27; ++i) { const int un = hw + 4 * i;
                if (i == 4) {
                    asm volatile("s_waitcnt vmcnt(4)" ::: "memory"); if (lane == 0) __hip_atomic_fetch_add(W + W_HLAND, 1u, __ATOMIC_RELAXED, __HIP_MEMORY_SCOPE_WORKGROUP); }
                if (i < 18) { const int rem = un - (i < 9 ? 0 : 36), ct = rem >= 18, Sx = rem - 18 * ct; u[i] = __builtin_amdgcn_raw_buffer_load_b128(i < 9 ? rg_ : ru_, vgu, Sx * 32 * F * 2 + ct * 32, 0); }
                else u[i] = __builtin_amdgcn_raw_buffer_load_b128(rd_, vd, (un - 72) * 32, 0); }
            LAS char* slot = (LAS char*)lds + SP + 128 + hw * 1024; const int row = lane >> 1;
            LAS char* wa = slot + ((row & ~12) | ((row & 4) << 1) | ((row & 8) >> 1)) * 32 + (lane & 1) * 16;
            LAS const char* ra = slot + (16 * (gq >> 1) + 4 * (gq & 1) + (r16 >> 2)) * 32 + (r16 & 3) * 8;
            const unsigned dst = (unsigned)WS_WF + (fc * 108 + hw) * 1024 + lane * 16;
#pragma unroll
            for (int Sx = 0; Sx < 18; ++Sx) PIN(cv[Sx]);
#pragma unroll
            for (int i0 = 0; i0 < 27; i0 += 9) { s16x4 lo[9], hi[9];
#pragma unroll
                for (int j = 0; j < 9; ++j) { asm volatile("" ::: "memory"); *(LAS u32x4*)wa = u[i0 + j]; asm volatile("" ::: "memory"); lo[j] = trread(ra); hi[j] = trread(ra + 256); }
#pragma unroll
                for (int j = 0; j < 9; ++j) __builtin_amdgcn_raw_buffer_store_b128(__builtin_bit_cast(u32x4, __builtin_shufflevector(lo[j], hi[j], 0, 1, 2, 3, 4, 5, 6, 7)), rws, dst, (i0 + j) * 4096, 16); }
        }
        STAMP(1, 2);
        float csv = 0.f, wsum_ = 0.f;
#pragma unroll
        for (int Sx = 0; Sx < 18; ++Sx) csv += __uint_as_float(cv[Sx]);
        if (hw == 2) {
#pragma unroll
            for (int i = 0; i < 5; ++i) wsum_ = dot2bf(cv[i], 0x3F803F80u, wsum_);
            wsum_ = wave_sum(wsum_); }
        STAMP(1, 3);
        BAR_LDS();
        STAMP(1, 4);
        if (__builtin_expect(any_bad(lds), 0)) { fallback_restore(c, kv, false); BAR_LDS(); BAR_LDS(); BAR_LDS(); }
        if (hw < 2 && hw * 64 + lane < 80) cs[hw * 64 + lane] = csv;
        if (hw == 2) {
#pragma unroll
            for (int i = 0; i < 5; ++i) if (i * 64 + lane < 288) ((LAS unsigned*)(lds + 29696))[i * 64 + lane] = cv[i];
            if (lane == 0) W[W_WSUM] = __float_as_uint(wsum_); }
        BAR_LDS();
        STAMP(1, 5);
        if (hw != 0) stage_frags(c, p.ws + WS_WOF + (size_t)m * FRAGS_B, hw - 1, 3); else wait_flags8(flags, 1u, lane, (LAS unsigned*)(lds + 126976));
        STAMP(1, 6);
        asm volatile("s_barrier" ::: "memory");
        BAR_ALL();
        BAR_LDS();
        asm volatile("s_barrier" ::: "memory");
        if (hw != 0) stage_frags(c, p.ws + WS_WQF + (size_t)m * FRAGS_B, hw - 1, 3); else wait_flags8(flags + 32, 1u, lane, (LAS unsigned*)(lds + 126976 + 1024));
        asm volatile("s_barrier" ::: "memory");
        BAR_ALL();
        BAR_LDS();
        STAMP_FLUSH(1);
        return;
    }
    FragQ q; load_q_stage1(c, p.ws + WS_QF + ((size_t)c.b * 8 + c.pp * 2) * 1152, kv, q, W);
    lds_sync(W + W_ASYNC, 8u, lane);
    STAMP(1, 1); CYC(14);
    f32x16 oa = {}, ob = {};
    attn_pass((LAS const char*)lds, wave, lane, q, oa, ob, W + W_ASYNC);
    STAMP(1, 2); CYC(15);
    { const bool bad = __any(o_bad(oa, c.h) || o_bad(ob, c.h)); if (lane == 0) W[W_BAD + wave] = bad; put_partial(lds, c, oa, ob); }
    BAR_LDS();
    if (__builtin_expect(any_bad(lds), 0)) { fallback_restore(c, kv, true); fallback_pass(lds, c, q, oa, ob); }
    STAMP(1, 3);
    { f32x4 a, e; f32x2 z; finish_o(lds, c, oa, ob, a, e, z); publish_o(rws, (unsigned)WS_XO + g * XO_G, c, a, e, z); }
    STAMP(1, 4);
    BAR_LDS();
    if (tid == 0) __hip_atomic_store(flags + m * 64, 1u, RLX_AGENT);
    const unsigned xo = (unsigned)WS_XO + g * XO_G, xx = (unsigned)WS_XX + g * XO_G;
    const unsigned aoff = ((4 * 9 * kh + gq) * 64 + rt * 16 + r16) * 16;
    unsigned xxo[3]; u32x2 resb[3];
#pragma unroll
    for (int i = 0; i < 3; ++i) { const int ct = kh == 0 ? i : 3 + i, n = 16 * ct + 4 * gq;
        resb[i] = *(const u32x2*)(p.x + (size_t)T2 * D + 72 * m + (n < 72 ? n : 68));
        xxo[i] = n < 72 ? xx + ((9 * m + 2 * ct + (gq >> 1)) * 64 + rt * 16 + r16) * 16 + (gq & 1) * 8 : OOB; PIN(xxo[i]); }
    QkvOff qo = qkv_offsets(WS_QF + QF_B, WS_KV + KVP_B, c.b, m, T2, kh, gq); PIN_QKV(qo);
    asm volatile("s_barrier" ::: "memory");
    STAMP(1, 5);
    bf16x8 af[9];
    { u32x4 x[9]; load_checked(x, rws, aoff, xo, 4096);
#pragma unroll
      for (int s = 0; s < 9; ++s) af[s] = __builtin_bit_cast(bf16x8, x[s]); }
    BAR_ALL();
    STAMP(1, 6);
    LAS const char* bb = (LAS const char*)lds + B1_OFF + kh * 45 * 1024 + lane * 16;
    auto ldb = [&](int s, int ct) { return *(LAS const bf16x8*)(bb + (s * 5 + ct) * 1024); };
    f32x4 x1[3];
    { f32x4 acc[5] = {}; gemm45(acc, af, ldb); STAMP(1, 7); khalf_reduce((LAS f32x4*)lds, acc, x1, rt, kh, lane); }
#pragma unroll
    for (int i = 0; i < 3; ++i) x1[i] += f32x4{bflo(resb[i][0]), bfhi(resb[i][0]), bflo(resb[i][1]), bfhi(resb[i][1])};
#pragma unroll
    for (int i = 0; i < 3; ++i) { u32x2 w = {nopend(pk2(x1[i][0], x1[i][1])), nopend(pk2(x1[i][2], x1[i][3]))}; __builtin_amdgcn_raw_buffer_store_b64(w, rws, xxo[i], 0, 16); }
    BAR_LDS();
    if (tid == 0) __hip_atomic_store(flags + m * 64 + 32, 1u, RLX_AGENT);
    asm volatile("s_barrier" ::: "memory");
    STAMP(1, 8);
    bf16x8 xf[9];
    { u32x4 x[9]; load_checked(x, rws, aoff, xx, 4096);
#pragma unroll
      for (int s = 0; s < 9; ++s) xf[s] = __builtin_bit_cast(bf16x8, x[s]); }
    BAR_ALL();
    STAMP(1, 9);
    float sum, sq, dot = 0.f;
    { u32x4 xa[9];
#pragma unroll
      for (int s = 0; s < 9; ++s) xa[s] = __builtin_bit_cast(u32x4, xf[s]);
      ln_sums(xa, sum, sq);
      LAS const char* wrl = (LAS const char*)lds + 29696 + kh * 576 + gq * 16;
#pragma unroll
      for (int s = 0; s < 9; ++s) { const u32x4 wv = *(LAS const u32x4*)(wrl + s * 64);
#pragma unroll
          for (int j = 0; j < 4; ++j) dot = dot2bf(xa[s][j], wv[j], dot); }
      dot = xg_sum(dot); }
    LAS f32x4* st = (LAS f32x4*)(lds + 20480);
    if (gq == 0) st[wave * 16 + r16] = f32x4{sum, sq, dot, 0.f};
    SBAR();
    f32x4 acc[5] = {}; gemm45(acc, xf, ldb);
    STAMP(1, 10);
    f32x4 o3[3]; khalf_reduce((LAS f32x4*)lds, acc, o3, rt, kh, lane);
    { const f32x4 o = st[(wave ^ 4) * 16 + r16]; sum += o[0]; sq += o[1]; }
    const float mu = sum * (1.f / D), var = fmaxf(sq * (1.f / D) - mu * mu, 0.f), rs = rsqrtf(var + 1e-5f);
    const float sc = (1.f + rs) * rsqrtf(var * (1.f + rs) * (1.f + rs) + 1e-5f);
#pragma unroll
    for (int i = 0; i < 3; ++i) o3[i] = (o3[i] - mu * *(LAS const f32x4*)(cs + (kh == 0 ? 16 * i : (i < 2 ? 48 + 16 * i : 64)) + 4 * gq)) * sc;
    store_qkv3(rws, qo, WS_KV + KVP_B, m, g, tid, o3);
    if (m == 0) { u32x2 v = {__float_as_uint(mu), __float_as_uint(rs)}; __builtin_amdgcn_raw_buffer_store_b64(v, rws, (kh == 0 && gq == 0) ? (unsigned)WS_ST + T2 * 8 : OOB, 0, 0);
        if (wave == 7) { const f32x4 a = st[(lane >> 4) * 16 + (lane & 15)], e = st[((lane >> 4) + 4) * 16 + (lane & 15)];
            float best; const int i = wave_argmax(router_score(a[0] + e[0], a[1] + e[1], a[2] + e[2], __uint_as_float(W[W_WSUM])), best);
            if (lane == 0) { u32x2 v2 = {__float_as_uint(best), (unsigned)(g * 64 + i)}; __builtin_amdgcn_raw_buffer_store_b64(v2, rws, (unsigned)WS_CAND0 + g * 8, 0, 0); } } }
    STAMP(1, 11);
}

struct FfnW { u32x4 a[18], d[9]; };
DI void ffn_load(FfnW& w, rsrc_t rws, int c, int hw, int lane) {
    const unsigned base = (unsigned)WS_WF + c * 108 * 1024;
#pragma unroll
    for (int i = 0; i < 18; ++i) w.a[i] = __builtin_amdgcn_raw_buffer_load_b128(rws, lane * 16 + hw * 18 * 1024, base + i * 1024, 0);
#pragma unroll
    for (int i = 0; i < 9; ++i) w.d[i] = __builtin_amdgcn_raw_buffer_load_b128(rws, lane * 16 + hw * 9 * 1024, base + (72 + i) * 1024, 0);
}
DI void ffn_partial(const FfnW& w, char* lds, char* ws, rsrc_t rws, int use, int c, int t, int hw, int lane, unsigned hs0, LAS unsigned* landed) {
    LAS float* red = (LAS float*)(lds + SP_RED); LAS u16* hv_ = (LAS u16*)(lds + SP_HS) + hw * 32;
    LAS unsigned* hcnt = (LAS unsigned*)(lds + SP_W) + W_HSYNC; const int gq = lane >> 4;
    LAS const char* xs = (LAS const char*)lds + SP_XS + gq * 16;
    f32x4 a0 = {0, 0, 0, 0}, a1 = {0, 0, 0, 0};
#pragma unroll
    for (int i = 0; i < 18; i += 2) { a0 = MFMA16(__builtin_bit_cast(bf16x8, w.a[i]), *(LAS const bf16x8*)(xs + i * 64), a0); a1 = MFMA16(__builtin_bit_cast(bf16x8, w.a[i + 1]), *(LAS const bf16x8*)(xs + (i + 1) * 64), a1); }
    if ((lane & 15) == 0) *(LAS f32x4*)(red + hw * 16 + gq * 4) = a0 + a1;
    if (landed) { asm volatile("s_waitcnt vmcnt(0)" ::: "memory"); if (lane == 0) __hip_atomic_fetch_add(landed, 1u, __ATOMIC_RELAXED, __HIP_MEMORY_SCOPE_WORKGROUP); }
    lds_sync(hcnt, hs0 + 4, lane);
    { const float g = red[lane & 31], u = red[32 + (lane & 31)]; if (lane < 32) hv_[lane] = f2bf(g * __builtin_amdgcn_rcpf(1.f + __expf(-g)) * u); }
    asm volatile("s_waitcnt lgkmcnt(0)" ::: "memory");
    const bf16x8 hf = *(LAS const bf16x8*)(hv_ + 8 * gq);
    float y[9];
#pragma unroll
    for (int k = 0; k < 9; ++k) y[k] = MFMA16(hf, __builtin_bit_cast(bf16x8, w.d[k]), (f32x4{0, 0, 0, 0}))[0];
    const unsigned so = (unsigned)WS_SLAB + ((use * 144 + c) * 576 + hw * 144 + lane) * 4;
#pragma unroll
    for (int rd = 0; rd < 3; ++rd) { const float v = rd < 2 ? (gq == 0 ? y[4 * rd] : gq == 1 ? y[4 * rd + 1] : gq == 2 ? y[4 * rd + 2] : y[4 * rd + 3]) : y[8];
        __builtin_amdgcn_raw_buffer_store_b32(nopendf(v), rws, (rd < 2 || gq == 0) ? so + rd * 256 : OOB, 0, 16); }
    lds_sync(hcnt, hs0 + 8, lane);
    if (t == 0) __hip_atomic_store((GAS unsigned*)(ws + WS_FF + (use * 144 + c) * 128), 1u, RLX_AGENT);
}
DI f32x4 ffn_reduce(char* ws, rsrc_t rws, int use, int rho, int hw, int lane, bool nap) {
    { unsigned sp = 0; for (;;) { bool ok = true;
#pragma unroll
        for (int i = 0; i < 3; ++i) { const int c = i * 64 + lane; ok &= __hip_atomic_load((GAS unsigned*)(ws + WS_FF + (use * 144 + (c < 144 ? c : 0)) * 128), RLX_AGENT) != 0; }
        if (__all(ok) || ++sp > (1u << 20)) break; if (nap) __builtin_amdgcn_s_sleep(2); } }
    f32x4 a = {0, 0, 0, 0}; u32x4 v[5];
    for (unsigned sp = 0;; ++sp) {
        asm volatile("" ::: "memory");
#pragma unroll
        for (int i = 0; i < 5; ++i) { const int c = (lane >> 1) + 32 * i; v[i] = __builtin_amdgcn_raw_buffer_load_b128(rws, c < 144 ? (unsigned)WS_SLAB + ((use * 144 + c) * 576 + rho * 32 + hw * 8 + (lane & 1) * 4) * 4 : OOB, 0, 16); }
        if (!any_pending(v) || sp > (1u << 16)) break; }
#pragma unroll
    for (int i = 0; i < 5; ++i) a += __builtin_bit_cast(f32x4, v[i]);
#pragma unroll
    for (int k = 0; k < 4; ++k) a[k] = xg_sum(dpp_add<0x128>(dpp_add<0x124>(dpp_add<0x122>(a[k]))));
    return a;
}
DI float sigmoidf_(float x) { return __builtin_amdgcn_rcpf(1.f + __expf(-x)); }
DI void patch_key(char* lds, int sel, int lane) {
    LAS const float* kn = (LAS const float*)(lds + SP_KVN); LAS char* t = (LAS char*)lds + (sel >> 5) * TILE_B; const int rr = sel & 31;
    if (lane < 16) *(LAS u16*)(t + ((lane >> 3) * 32 + rr) * 16 + (lane & 7) * 2) = f2bf(kn[lane]);
    else if (lane < 18) *(LAS u16*)(t + K2_OFF + rr * 4 + (lane - 16) * 2) = f2bf(kn[lane]);
    else if (lane < 36) *(LAS u16*)(t + V_OFF + (rr >> 3) * VSUB + (lane - 18) * 16 + (rr & 7) * 2) = f2bf(kn[lane]);
}

__global__ __launch_bounds__(768) void k_attn1(Params p) {
    extern __shared__ __attribute__((aligned(16))) char lds[];
    const AttnCtx c = attn_ctx(lds);
    const int tid = c.tid, lane = c.lane, wave = c.wave, kh = c.kh, m = c.m, g = c.g, rt = c.rt, r16 = c.r16, gq = c.gq, T2 = c.T2, r = c.r, h = c.h;
    STAMP_DECL; STAMP(2, 0);
    LAS unsigned* W = (LAS unsigned*)(lds + SP_W);
    if (tid < 32) W[tid] = 0;
    if (wave == 8) *(LAS u32x4*)(lds + SP_T + lane * 16) = u32x4{0, 0, 0, 0};
    BAR_LDS();
    const char* kv = p.ws + WS_KV + KVP_B;
    const rsrc_t rws = mkrs(p.ws, WS_END);
    GAS unsigned* flags = (GAS unsigned*)(p.ws + WS_FLAG) + (size_t)g * 8 * 64;
    const unsigned xo = (unsigned)WS_XO1 + g * XO_G;
    if (wave < 8) {
        FragQ q; load_q_stage1(c, p.ws + WS_QF + QF_B + ((size_t)c.b * 8 + c.pp * 2) * 1152, kv, q, W);
        lds_sync(W + W_ASYNC, 8u, lane);
        STAMP(2, 1); CYC(14);
        f32x16 oa = {}, ob = {};
        attn_pass((LAS const char*)lds, wave, lane, q, oa, ob, W + W_ASYNC);
        STAMP(2, 2); CYC(15);
        { unsigned sp = 0; while (__builtin_amdgcn_readfirstlane(__hip_atomic_load(W + W_READY, __ATOMIC_RELAXED, __HIP_MEMORY_SCOPE_WORKGROUP)) == 0 && ++sp < (1u << 24)) __builtin_amdgcn_s_sleep(1); }
        asm volatile("" ::: "memory");
        const int sel = __builtin_amdgcn_readfirstlane(W[W_SEL]); const bool owner = (sel >> 6) == g;
        bool bad;
        { LAS const char* sp = (LAS const char*)lds + SP_T + (kh == ((sel >> 5) & 1) ? 0 : 512);
          const bf16x8 k1 = *(LAS const bf16x8*)(sp + (r == 0 ? h * 16 : 48)); u32x4 k2u = {*(LAS const unsigned*)(sp + (r == 0 ? 32 : 48)), 0, 0, 0};
          const bf16x8 v0 = *(LAS const bf16x8*)(sp + (h == 0 ? 64 + (r < 19 ? r : 19) * 16 : 48)); const f32x16 zero = {};
          f32x16 sa = MFMA32(k1, q.a1, zero); sa = MFMA32(__builtin_bit_cast(bf16x8, k2u), q.a2, sa);
          f32x16 sb = MFMA32(k1, q.b1, zero); sb = MFMA32(__builtin_bit_cast(bf16x8, k2u), q.b2, sb);
#pragma unroll
          for (int j = 0; j < 8; ++j) { sa[j] = __builtin_amdgcn_exp2f(sa[j]); sb[j] = __builtin_amdgcn_exp2f(sb[j]); }
          oa = pv(v0, lo8(sa), oa); ob = pv(v0, lo8(sb), ob);
          bad = o_bad(oa, h) || o_bad(ob, h) || (h == 0 && (!(oa[10] * 1024.f >= sa[0]) || !(ob[10] * 1024.f >= sb[0]))); }
        if (owner) { unsigned sp = 0;
            while (__builtin_amdgcn_readfirstlane(__hip_atomic_load(W + W_OZDONE, __ATOMIC_RELAXED, __HIP_MEMORY_SCOPE_WORKGROUP)) == 0 && ++sp < (1u << 24)) __builtin_amdgcn_s_sleep(1);
            asm volatile("" ::: "memory"); }
        { const bool anyb = __any(bad); if (lane == 0) W[W_BAD + wave] = anyb; put_partial(lds, c, oa, ob); }
        BAR_LDS();
        bool use_oz = owner;
        if (__builtin_expect(any_bad(lds) || W[W_OZBAD] != 0, 0)) {
            fallback_restore(c, kv, true);
            if (wave == 0) patch_key(lds, sel, lane);
            if (owner && r == (sel & 31)) { LAS const float* qn = (LAS const float*)(lds + SP_QN) + c.pp * 18; u32x4 a, z = {0, 0, 0, 0};
                for (int j = 0; j < 4; ++j) a[j] = pk2(qn[8 * h + 2 * j], qn[8 * h + 2 * j + 1]);
                z[0] = h == 0 ? pk2(qn[16], qn[17]) : 0u;
                if (((sel >> 5) & 1) == 0) { q.a1 = __builtin_bit_cast(bf16x8, a); q.a2 = __builtin_bit_cast(bf16x8, z); } else { q.b1 = __builtin_bit_cast(bf16x8, a); q.b2 = __builtin_bit_cast(bf16x8, z); } }
            BAR_LDS();
            fallback_pass(lds, c, q, oa, ob);
            use_oz = false;
        }
        STAMP(2, 3);
        { f32x4 a, e; f32x2 z; finish_o(lds, c, oa, ob, a, e, z);
          if (use_oz && kh == ((sel >> 5) & 1) && r == (sel & 31)) { LAS const float* oz = (LAS const float*)(lds + SP_OZ) + c.pp * 20;
              a = *(LAS const f32x4*)(oz + 4 * h); e = *(LAS const f32x4*)(oz + 8 + 4 * h); z = f32x2{oz[16], oz[17]}; }
          publish_o(rws, xo, c, a, e, z); }
        STAMP(2, 4);
        BAR_LDS();
        if (tid == 0) __hip_atomic_store(flags + m * 64, 2u, RLX_AGENT);
        const unsigned aoff = ((4 * 9 * kh + gq) * 64 + rt * 16 + r16) * 16;
        unsigned coff[3]; f32x4 res[3]; u32x2 wrp[3];
        { const u32x2 stt = __builtin_amdgcn_raw_buffer_load_b64(rws, T2 * 8, (unsigned)WS_ST, 0); const float mu1 = __uint_as_float(stt[0]), rs1 = __uint_as_float(stt[1]);
#pragma unroll
          for (int i = 0; i < 3; ++i) { const int ct = kh == 0 ? i : 3 + i, n0 = 16 * ct + 4 * gq, n = n0 < 72 ? n0 : 68;
            const u32x2 xb = __builtin_amdgcn_raw_buffer_load_b64(rws, ((9 * m + (n >> 3)) * 64 + rt * 16 + r16) * 16 + ((n >> 2) & 1) * 8, (unsigned)WS_XX + g * XO_G, 0);
            const f32x4 x1 = {bflo(xb[0]), bfhi(xb[0]), bflo(xb[1]), bfhi(xb[1])};
            res[i] = x1 + (x1 - mu1) * rs1;
            if (T2 == sel) res[i] = *(LAS const f32x4*)((LAS const float*)(lds + SP_XS) + 72 * m + n);
            wrp[i] = *(const u32x2*)(p.wr + 72 * m + n);
            coff[i] = n0 < 72 ? T2 * D + 72 * m + n0 : OOB / 2; PIN(coff[i]); } }
        asm volatile("s_barrier" ::: "memory");
        STAMP(2, 5);
        bf16x8 af[9];
        { u32x4 x[9]; load_checked(x, rws, aoff, xo, 4096);
#pragma unroll
          for (int s = 0; s < 9; ++s) af[s] = __builtin_bit_cast(bf16x8, x[s]); }
        BAR_ALL();
        STAMP(2, 6);
        LAS const char* bb = (LAS const char*)lds + B1_OFF + kh * 45 * 1024 + lane * 16;
        f32x4 x3[3];
        { f32x4 acc[5] = {}; gemm45(acc, af, [&](int s, int ct) { return *(LAS const bf16x8*)(bb + (s * 5 + ct) * 1024); }); STAMP(2, 7);
          khalf_reduce((LAS f32x4*)lds, acc, x3, rt, kh, lane); }
        float sx = 0.f, sq = 0.f, sw = 0.f;
#pragma unroll
        for (int i = 0; i < 3; ++i) { x3[i] += res[i]; const bool ok = coff[i] < OOB / 2;
            const f32x4 x = ok ? x3[i] : f32x4{0, 0, 0, 0};
            sx += (x[0] + x[1]) + (x[2] + x[3]); sq += x[0] * x[0] + x[1] * x[1] + x[2] * x[2] + x[3] * x[3];
            sw += x[0] * bflo(wrp[i][0]) + x[1] * bfhi(wrp[i][0]) + x[2] * bflo(wrp[i][1]) + x[3] * bfhi(wrp[i][1]); }
        sx = xg_sum(sx); sq = xg_sum(sq); sw = xg_sum(sw);
        LAS float* st = (LAS float*)(lds + 24576);
        if (gq == 0) *(LAS f32x4*)(st + (kh * 64 + rt * 16 + r16) * 4) = f32x4{sx, sq, sw, 0.f};
        BAR_LDS();
        if (tid < 192) { const int row = (tid * 171) >> 9, cp = tid - row * 3;
            put_granule(p.ws, (unsigned)WS_GST + ((g * 8 + m) * 192 + tid) * 8, __float_as_uint(st[row * 4 + cp] + st[(64 + row) * 4 + cp]));
            unsigned v[8]; get_granules<8>(p.ws, (unsigned)WS_GST + (g * 8 * 192 + tid) * 8, 192 * 8, true, v, false);
            float t_ = 0.f;
#pragma unroll
            for (int k = 0; k < 8; ++k) t_ += __uint_as_float(v[k]);
            st[512 + row * 4 + cp] = t_; }
        BAR_LDS();
        STAMP(2, 8);
        const float wsum = __uint_as_float(W[W_WSUM]);
        int best; float bestr;
        { const f32x4 t = *(LAS const f32x4*)(st + 512 + lane * 4); best = wave_argmax(router_score(t[0], t[1], t[2], wsum), bestr); }
        if (m == 0 && wave == 0 && lane < 8)
            __hip_atomic_store((GAS unsigned long long*)(p.ws + WS_GCAND + (lane * 32 + g) * 8), ((unsigned long long)(0x100u | best) << 32) | __float_as_uint(bestr), RLX_AGENT);
        const f32x4 t = *(LAS const f32x4*)(st + 512 + (rt * 16 + r16) * 4);
        const float mu = t[0] * (1.f / D), rs = rsqrtf(fmaxf(t[1] * (1.f / D) - mu * mu, 0.f) + 1e-5f);
        const bool cand = rt * 16 + r16 == best;
        const rsrc_t rout = mkrs(p.out, S * D * 2);
        f32x4 xn[3], x4[3];
#pragma unroll
        for (int i = 0; i < 3; ++i) { xn[i] = (x3[i] - mu) * rs; x4[i] = x3[i] + xn[i]; }
        if (rt == (best >> 4)) {
#pragma unroll
            for (int i = 0; i < 3; ++i) { const int n = 16 * (kh == 0 ? i : 3 + i) + 4 * gq;
                if (cand && n < 72) { const unsigned gx = (unsigned)WS_GCXN + ((g * 8 + m) * 36 + n / 2) * 8, g4 = (unsigned)WS_GCX4 + ((g * 8 + m) * 72 + n) * 8;
                    put_granule(p.ws, gx, pk2(xn[i][0], xn[i][1])); put_granule(p.ws, gx + 8, pk2(xn[i][2], xn[i][3]));
                    for (int j = 0; j < 4; ++j) put_granule(p.ws, g4 + j * 8, __float_as_uint(x4[i][j])); } } }
#pragma unroll
        for (int i = 0; i < 3; ++i) { u32x2 w = {pk2(x4[i][0], x4[i][1]), pk2(x4[i][2], x4[i][3])};
            __builtin_amdgcn_raw_buffer_store_b64(w, rout, (coff[i] < OOB / 2 && !cand) ? coff[i] * 2 : OOB, 0, 0); }
        STAMP(2, 9);
        STAMP_FLUSH(2);
        return;
    }
    __builtin_amdgcn_s_setprio(3);
    const int t = tid - 512, hw = wave - 8;
    LAS unsigned* hcnt = W + W_HSYNC; unsigned hs = 0;
    const bool isF = (c.b >> 3) < 18, isR = !isF && ((c.b >> 3) - 18) * 8 + (c.b & 7) < 18;
    const int fc = (c.b & 7) * 18 + (c.b >> 3), rho = ((c.b >> 3) - 18) * 8 + (c.b & 7);
    const u32x2 cd = *(const u32x2*)(p.ws + WS_CAND0 + (lane & 31) * 8);
    int sel; float rsel;
    { const int i = wave_argmax(__uint_as_float(cd[0]), rsel); sel = __builtin_amdgcn_readlane((int)cd[1], i); }
    const bool owner = (sel >> 6) == g;
    const unsigned xrow0 = (unsigned)WS_XX + (sel >> 6) * XO_G + (sel & 63) * 16;
    const u32x2 stt = __builtin_amdgcn_raw_buffer_load_b64(rws, 0, (unsigned)WS_ST + sel * 8, 0);
    const u32x2 xb0 = __builtin_amdgcn_raw_buffer_load_b64(rws, t < 144 ? (t >> 1) * 1024 + (t & 1) * 8 : 0, xrow0, 0);
    FfnW w;
    while (__builtin_amdgcn_readfirstlane(__hip_atomic_load(W + W_ISSUED, __ATOMIC_RELAXED, __HIP_MEMORY_SCOPE_WORKGROUP)) < 8u) __builtin_amdgcn_s_sleep(1);
    if (isF) ffn_load(w, rws, fc, hw, lane);
    stage_rest(kv, c.lds0, hw, lane);
    STAMP(2, 1);
    const float mu1 = __uint_as_float(stt[0]), rs1 = __uint_as_float(stt[1]);
    if (isF) {
        if (t < 144) { u32x2 o = {pk2((bflo(xb0[0]) - mu1) * rs1, (bfhi(xb0[0]) - mu1) * rs1), pk2((bflo(xb0[1]) - mu1) * rs1, (bfhi(xb0[1]) - mu1) * rs1)}; *(LAS u32x2*)(lds + SP_XS + t * 8) = o; }
        STAMP(2, 2);
        lds_sync(hcnt, hs += 4, lane); ffn_partial(w, lds, p.ws, rws, 0, fc, t, hw, lane, hs, W + W_HLAND); hs += 8; }
    else { asm volatile("s_waitcnt vmcnt(0)" ::: "memory"); if (lane == 0) __hip_atomic_fetch_add(W + W_HLAND, 1u, __ATOMIC_RELAXED, __HIP_MEMORY_SCOPE_WORKGROUP); }
    if (hw == 3) { float s_ = 0.f; for (int i = 0; i < 9; ++i) s_ += bf2f(p.wr[i * 64 + lane]); s_ = wave_sum(s_); if (lane == 0) W[W_WSUM] = __float_as_uint(s_); }
    STAMP(2, 3);
    if (isR) { const int c0 = rho * 32 + hw * 8 + (lane & 1) * 4; const u32x2 xb = __builtin_amdgcn_raw_buffer_load_b64(rws, (c0 >> 3) * 1024 + (c0 & 7) * 2, xrow0, 0);
        f32x4 xo_ = {bflo(xb[0]), bfhi(xb[0]), bflo(xb[1]), bfhi(xb[1])}; xo_ += (xo_ - mu1) * rs1;
        const f32x4 y = ffn_reduce(p.ws, rws, 0, rho, hw, lane, true); const float sg = sigmoidf_(rsel);
        if (lane < 8) put_granule(p.ws, (unsigned)WS_GROW + (rho * 32 + hw * 8 + (lane & 1) * 4 + (lane >> 1)) * 8, __float_as_uint(xo_[lane >> 1] + y[lane >> 1] * sg)); }
    STAMP(2, 4);
    auto kv_tiles = [&]() {
        if (hw == 0) {
            unsigned v[1]; get_granules<1>(p.ws, (unsigned)WS_GKV + ((c.b & 15) * 64 + lane) * 8, 0, lane < 36, v, true);
            LAS float* kn = (LAS float*)(lds + SP_KVN); if (lane < 36) kn[lane] = __uint_as_float(v[0]);
            LAS char* sp = (LAS char*)lds + SP_T;
            LAS const char* kt = (LAS const char*)lds + (sel >> 5) * TILE_B; const int rr_ = sel & 31;
            if (lane < 16) { *(LAS u16*)(sp + lane * 2) = *(LAS const u16*)(kt + ((lane >> 3) * 32 + rr_) * 16 + (lane & 7) * 2); *(LAS u16*)(sp + 512 + lane * 2) = f2bf(kn[lane]); }
            else if (lane < 18) { *(LAS u16*)(sp + 32 + (lane - 16) * 2) = *(LAS const u16*)(kt + K2_OFF + rr_ * 4 + (lane - 16) * 2); *(LAS u16*)(sp + 512 + 32 + (lane - 16) * 2) = f2bf(kn[lane]); }
            else if (lane < 36) { const int d = lane - 18; *(LAS u16*)(sp + 64 + d * 16) = (u16)(*(LAS const u16*)(kt + V_OFF + (rr_ >> 3) * VSUB + d * 16 + (rr_ & 7) * 2) ^ 0x8000u); *(LAS u16*)(sp + 512 + 64 + d * 16) = f2bf(kn[lane]); }
            else if (lane == 36) { *(LAS u16*)(sp + 64 + 18 * 16) = 0xBF80u; *(LAS u16*)(sp + 512 + 64 + 18 * 16) = 0x3F80u; }
            if (lane == 0) W[W_SEL] = sel;
            asm volatile("s_waitcnt lgkmcnt(0)" ::: "memory");
            if (lane == 0) __hip_atomic_store(W + W_READY, 1u, __ATOMIC_RELAXED, __HIP_MEMORY_SCOPE_WORKGROUP);
        }
    };
    while (__builtin_amdgcn_readfirstlane(__hip_atomic_load(W + W_HLAND, __ATOMIC_RELAXED, __HIP_MEMORY_SCOPE_WORKGROUP)) < 4u ||
           __builtin_amdgcn_readfirstlane(__hip_atomic_load(W + W_ASYNC, __ATOMIC_RELAXED, __HIP_MEMORY_SCOPE_WORKGROUP)) < 8u) __builtin_amdgcn_s_sleep(4);
    asm volatile("" ::: "memory");
    if (owner) {
        u32x4 wf[5][5];
#pragma unroll
        for (int i = 0; i < 5; ++i)
#pragma unroll
            for (int ct = 0; ct < 5; ++ct) { const int Sx = hw + 4 * i < 18 ? hw + 4 * i : hw; wf[i][ct] = *(const u32x4*)(p.ws + WS_WQF + ((size_t)(m * 90 + Sx * 5 + ct) * 64 + lane) * 16); }
        LAS float* xrow = (LAS float*)(lds + SP_XS);
        unsigned v[9]; get_granules<9>(p.ws, (unsigned)WS_GROW + lane * 8, 64 * 8, true, v, false);
        float s1 = 0.f, s2 = 0.f;
#pragma unroll
        for (int i = 0; i < 9; ++i) { const float x = __uint_as_float(v[i]); xrow[i * 64 + lane] = x; s1 += x; s2 += x * x; }
        f32x4 acc[5] = {};
#pragma unroll
        for (int i = 0; i < 5; ++i) { const int Sx = hw + 4 * i; if (Sx < 18) { const f32x4 a = *(LAS const f32x4*)(xrow + 32 * Sx + 8 * gq), e = *(LAS const f32x4*)(xrow + 32 * Sx + 8 * gq + 4);
            u32x4 xb = {pk2(a[0], a[1]), pk2(a[2], a[3]), pk2(e[0], e[1]), pk2(e[2], e[3])};
            if (r16 != 0) xb = u32x4{0x3F803F80u, 0x3F803F80u, 0x3F803F80u, 0x3F803F80u};
#pragma unroll
            for (int ct = 0; ct < 5; ++ct) acc[ct] = MFMA16(__builtin_bit_cast(bf16x8, wf[i][ct]), __builtin_bit_cast(bf16x8, xb), acc[ct]); } }
        LAS float* scr = (LAS float*)(lds + SP_SCR);
        if (r16 < 2) {
#pragma unroll
            for (int ct = 0; ct < 5; ++ct) *(LAS f32x4*)(scr + (hw * 2 + r16) * 80 + ct * 16 + gq * 4) = acc[ct]; }
        s1 = wave_sum(s1); s2 = wave_sum(s2);
        const float mu = s1 * (1.f / D), rs = rsqrtf(fmaxf(s2 * (1.f / D) - mu * mu, 0.f) + 1e-5f);
        lds_sync(hcnt, hs += 4, lane);
        const int tt = hw == 1 ? 72 + (lane & 7) : (hw == 0 ? lane : 64 + (lane & 7));
        const float val = rs * ((scr[tt] + scr[160 + tt] + scr[320 + tt] + scr[480 + tt]) - mu * (scr[80 + tt] + scr[240 + tt] + scr[400 + tt] + scr[560 + tt]));
        if (hw == 1) { const int j = lane & 7, idx = m < 4 ? (m >> 1) * 18 + (m & 1) * 8 + j : ((m == 4 || m == 5) && j >= 6) ? (m - 4) * 18 + 10 + j : -1;
            if (idx >= 0) { put_granule(p.ws, (unsigned)WS_GKV + ((lane >> 3) * 64 + idx) * 8, __float_as_uint(val)); put_granule(p.ws, (unsigned)WS_GKV + (((lane >> 3) + 8) * 64 + idx) * 8, __float_as_uint(val)); } }
        else if (hw == 0 || (hw == 2 && lane < 8)) ((LAS float*)(lds + SP_QN))[tt] = val * QSCALE;
            STAMP(2, 5);
        lds_sync(hcnt, hs += 4, lane);
        f32x16 oz = {};
        { LAS const float* qn = (LAS const float*)(lds + SP_QN); const int hd = r < 2 ? r : (r == 4 || r == 5) ? r - 2 : -1;
          u32x4 a = {0, 0, 0, 0}, z = {0, 0, 0, 0};
          if (hd >= 0) { for (int j = 0; j < 4; ++j) a[j] = pk2(qn[hd * 18 + 8 * h + 2 * j], qn[hd * 18 + 8 * h + 2 * j + 1]); if (h == 0) z[0] = pk2(qn[hd * 18 + 16], qn[hd * 18 + 17]); }
          const bf16x8 qz1 = __builtin_bit_cast(bf16x8, a), qz2 = __builtin_bit_cast(bf16x8, z); const f32x16 zero = {}; const int dsel = r < 19 ? r : 19;
          LAS char* pz = (LAS char*)lds + SP_PZ + hw * 1024;
          LAS const char* zc = (LAS const char*)lds + SP_T + 48;
          LAS const char* pr0 = r < 4 ? pz + r * 64 + h * 16 : zc; const int pon = r < 4 ? 1 : 0;
          LAS char* pw = pz + 2 * h * 64 + r * 2;
          LAS const char* tk = (LAS const char*)lds + hw * 16 * TILE_B + lane * 16;
          LAS const char* tk2 = (LAS const char*)lds + hw * 16 * TILE_B + K2_OFF + r * 4;
          LAS const char* tv = (LAS const char*)lds + hw * 16 * TILE_B + V_OFF + h * VSUB + dsel * 16;
          const int kill = (r == (sel & 31)) ? (sel >> 5) - hw * 16 : -1;
          auto scores = [&](int j, f32x16& x, f32x16& y) { u32x4 ka = {*(LAS const unsigned*)(tk2 + 2 * j * TILE_B), 0, 0, 0}, kb = {*(LAS const unsigned*)(tk2 + (2 * j + 1) * TILE_B), 0, 0, 0};
              const bf16x8 k0 = *(LAS const bf16x8*)(tk + 2 * j * TILE_B), k1 = *(LAS const bf16x8*)(tk + (2 * j + 1) * TILE_B);
              x = MFMA32(qz1, k0, zero); y = MFMA32(qz1, k1, zero); x = MFMA32(qz2, __builtin_bit_cast(bf16x8, ka), x); y = MFMA32(qz2, __builtin_bit_cast(bf16x8, kb), y); };
          auto put_p = [&](int j, const f32x16& x, const f32x16& y) { float p0 = __builtin_amdgcn_exp2f(x[0]), p1 = __builtin_amdgcn_exp2f(x[1]), p2 = __builtin_amdgcn_exp2f(y[0]), p3 = __builtin_amdgcn_exp2f(y[1]);
              if (2 * j == kill) { p0 = 0.f; p1 = 0.f; } if (2 * j + 1 == kill) { p2 = 0.f; p3 = 0.f; }
              const unsigned pa = pk2(p0, p1), pb = pk2(p2, p3); LAS char* w_ = pw + (j & 1) * 512;
              asm volatile("" ::: "memory");
              *(LAS u16*)w_ = (u16)pa; *(LAS u16*)(w_ + 64) = (u16)(pa >> 16); *(LAS u16*)(w_ + 256) = (u16)pb; *(LAS u16*)(w_ + 320) = (u16)(pb >> 16);
              asm volatile("" ::: "memory"); };
          f32x16 oy = {};
          auto pvz = [&](int j) { LAS const char* b_ = pr0 + pon * (j & 1) * 512;
              const bf16x8 v0 = *(LAS const bf16x8*)(tv + 2 * j * TILE_B), v1 = *(LAS const bf16x8*)(tv + 2 * j * TILE_B + 2 * VSUB), v2 = *(LAS const bf16x8*)(tv + (2 * j + 1) * TILE_B), v3 = *(LAS const bf16x8*)(tv + (2 * j + 1) * TILE_B + 2 * VSUB);
              const bf16x8 b0 = *(LAS const bf16x8*)b_, b1 = *(LAS const bf16x8*)(b_ + pon * 32), b2 = *(LAS const bf16x8*)(b_ + pon * 256), b3 = *(LAS const bf16x8*)(b_ + pon * 288);
              oz = MFMA32(v0, b0, oz); oy = MFMA32(v2, b2, oy); oz = MFMA32(v1, b1, oz); oy = MFMA32(v3, b3, oy); };
          f32x16 xa, ya, xb, yb;
          scores(0, xa, ya); scores(1, xb, yb); put_p(0, xa, ya);
#pragma unroll
          for (int j = 1; j < 8; ++j) {
              if (j & 1) { if (j < 7) scores(j + 1, xa, ya); SBAR(); put_p(j, xb, yb); } else { if (j < 7) scores(j + 1, xb, yb); SBAR(); put_p(j, xa, ya); }
              SBAR(); pvz(j - 1); SBAR(); }
          pvz(7); oz += oy;
          STAMP(2, 6);
          kv_tiles();
          if (hw == 0) {
              LAS const char* sp = (LAS const char*)lds + SP_T + 512;
              u32x4 k2u = {*(LAS const unsigned*)(sp + (r == 0 ? 32 : 48)), 0, 0, 0};
              f32x16 sx = MFMA32(qz1, *(LAS const bf16x8*)(sp + (r == 0 ? h * 16 : 48)), zero); sx = MFMA32(qz2, __builtin_bit_cast(bf16x8, k2u), sx);
              const unsigned pp_ = pk2(__builtin_amdgcn_exp2f(sx[0]), __builtin_amdgcn_exp2f(sx[1]));
              asm volatile("" ::: "memory");
              *(LAS u16*)pw = (u16)pp_; *(LAS u16*)(pw + 64) = (u16)(pp_ >> 16);
              asm volatile("" ::: "memory");
              oz = MFMA32(*(LAS const bf16x8*)(sp + (h == 0 ? 64 + dsel * 16 : 48)), *(LAS const bf16x8*)pr0, oz); } }
        LAS float* sc2 = (LAS float*)(lds + SP_PZ);
        if (r < 4) {
#pragma unroll
            for (int j = 0; j < 11; ++j) sc2[hw * 256 + j * 8 + h * 4 + r] = oz[j]; }
        lds_sync(hcnt, hs += 4, lane);
        if (hw == 0) { LAS float* ozf = (LAS float*)(lds + SP_OZ); const int hd2 = lane & 3; bool bad = false;
            const float lsum = sc2[80 + hd2] + sc2[256 + 80 + hd2] + sc2[512 + 80 + hd2] + sc2[768 + 80 + hd2];
            bad |= !(lsum >= 0x1p-60f && lsum <= 0x1p59f); const float il = 1.f / lsum; float o2[2];
#pragma unroll
            for (int rd = 0; rd < 2; ++rd) { const int d = rd * 16 + (lane >> 2), dd = d < 18 ? d : 0, reg = (dd & 3) + 4 * (dd >> 3), hh = (dd >> 2) & 1, o_ = reg * 8 + hh * 4 + hd2;
                const float s_ = sc2[o_] + sc2[256 + o_] + sc2[512 + o_] + sc2[768 + o_]; bad |= !(fabsf(s_) < INFINITY); o2[rd] = s_ * il; }
            asm volatile("" ::: "memory");
            ozf[hd2 * 20 + (lane >> 2)] = o2[0]; if ((lane >> 2) < 2) ozf[hd2 * 20 + 16 + (lane >> 2)] = o2[1];
            if (__any(bad) && lane == 0) W[W_OZBAD] = 1;
            asm volatile("s_waitcnt lgkmcnt(0)" ::: "memory");
            if (lane == 0) __hip_atomic_store(W + W_OZDONE, 1u, __ATOMIC_RELAXED, __HIP_MEMORY_SCOPE_WORKGROUP); }
            if (isF) ffn_load(w, rws, fc, hw, lane); else w = FfnW{};
    } else kv_tiles();
    STAMP(2, 7);
    BAR_LDS();
    if (__builtin_expect(any_bad(lds) || W[W_OZBAD] != 0, 0)) { fallback_restore(c, kv, false); BAR_LDS(); BAR_LDS(); BAR_LDS(); BAR_LDS(); }
    asm volatile("s_waitcnt lgkmcnt(0)\n\ts_barrier" ::: "memory");
    if (hw != 0) stage_frags(c, p.ws + WS_WOF + (size_t)m * FRAGS_B, hw - 1, 3); else wait_flags8(flags, 2u, lane, (LAS unsigned*)(lds + 126976));
    asm volatile("s_barrier" ::: "memory");
    BAR_ALL();
    asm volatile("s_barrier\n\ts_barrier\n\ts_barrier" ::: "memory");
    int sel2, crow; float rsel2;
    { unsigned long long x; unsigned sp = 0;
      for (;;) { x = ld_granule(p.ws, (unsigned)WS_GCAND + ((c.b & 7) * 32 + (lane & 31)) * 8); if (__all((x >> 32) != 0) || ++sp > (1u << 20)) break; }
      const int gi = wave_argmax(__uint_as_float((unsigned)x), rsel2) & 31; const int row = (int)((x >> 32) & 63);
      sel2 = gi * 64 + __builtin_amdgcn_readlane(row, gi); crow = g * 64 + __builtin_amdgcn_readlane(row, g); }
    const int g2 = sel2 >> 6;
    STAMP(2, 11);
    if (isF) {
        unsigned v[2]; unsigned sp = 0; const unsigned o0 = (unsigned)WS_GCXN + (g2 * 288 + t) * 8, o1 = t < 32 ? o0 + 256 * 8 : o0;
        for (;;) { const unsigned long long a = ld_granule(p.ws, o0), e = ld_granule(p.ws, o1); v[0] = (unsigned)a; v[1] = (unsigned)e; if (__all((a >> 32) && (e >> 32)) || ++sp > (1u << 20)) break; }
        LAS unsigned* xs = (LAS unsigned*)(lds + SP_XS); xs[t] = v[0]; if (t < 32) xs[256 + t] = v[1];
        lds_sync(hcnt, hs += 4, lane);
        ffn_partial(w, lds, p.ws, rws, 1, fc, t, hw, lane, hs, nullptr); hs += 8;
    }
    if (hw == 3 && crow != sel2) {
        unsigned v[2]; get_granules<2>(p.ws, (unsigned)WS_GCX4 + ((g * 8 + m) * 72 + 2 * lane) * 8, 8, lane < 36, v, false);
        if (lane < 36) *(unsigned*)(p.out + (size_t)crow * D + 72 * m + 2 * lane) = pk2(__uint_as_float(v[0]), __uint_as_float(v[1])); }
    STAMP(2, 12);
    if (isR) { unsigned v[1]; const int col = rho * 32 + hw * 8 + (lane & 1) * 4 + ((lane >> 1) & 3), mm = col / 72;
        get_granules<1>(p.ws, (unsigned)WS_GCX4 + ((g2 * 8 + mm) * 72 + col - mm * 72) * 8, 0, lane < 8, v, false);
        const f32x4 y = ffn_reduce(p.ws, rws, 1, rho, hw, lane, false);
        if (lane < 8) p.out[(size_t)sel2 * D + col] = f2bf(__uint_as_float(v[0]) + y[lane >> 1] * sigmoidf_(rsel2)); }
    STAMP(2, 13);
    STAMP_FLUSH(2);
}

extern "C" void kernel_launch(void* const* d_in, const int* in_sizes, int n_in, void* d_out, int out_size, void* d_ws, size_t ws_size, hipStream_t stream) {
    Params p; p.x = (const u16*)d_in[0]; p.wq = (const u16*)d_in[1]; p.wk = (const u16*)d_in[2]; p.wv = (const u16*)d_in[3]; p.wo = (const u16*)d_in[4];
    p.wr = (const u16*)d_in[6]; p.wg = (const u16*)d_in[7]; p.wu = (const u16*)d_in[8]; p.wd = (const u16*)d_in[9]; p.out = (u16*)d_out; p.ws = (char*)d_ws;
    (void)hipFuncSetAttribute((const void*)k_attn0, hipFuncAttributeMaxDynamicSharedMemorySize, ATT_LDS);
    (void)hipFuncSetAttribute((const void*)k_attn1, hipFuncAttributeMaxDynamicSharedMemorySize, ATT_LDS);
    (void)hipFuncSetAttribute((const void*)k_qkv, hipFuncAttributeMaxDynamicSharedMemorySize, QKV_LDS);
    k_qkv<<<256, 768, QKV_LDS, stream>>>(p);
    k_attn0<<<256, 768, ATT_LDS, stream>>>(p);
    k_attn1<<<256, 768, ATT_LDS, stream>>>(p);
}
```
